```python
import torch

def _softmax(x, axis=-1):
    return torch.softmax(x, dim=axis)
def _gelu(x):
    return torch.nn.functional.gelu(x, approximate="tanh")
def _silu(x):
    return torch.nn.functional.silu(x)
def _sigmoid(x):
    return torch.sigmoid(x)
def _layernorm(x, axis=-1, eps=1e-5):
    m = x.mean(dim=axis, keepdim=True)
    v = x.var(dim=axis, keepdim=True, unbiased=False)
    return (x - m) / torch.sqrt(v + eps)
def _scatter_add(tgt, idx, src, axis=0):
    return tgt.clone().index_add_(axis, idx, src)
def _segment_sum(src, seg, n):
    out = torch.zeros((n,) + src.shape[1:], dtype=src.dtype)
    return out.index_add_(0, seg, src)
def _pad(x, pw):
    flat = tuple(v for pair in reversed(pw) for v in pair)
    return torch.nn.functional.pad(x, flat)

kernel_name = 'concat_heads__mha_block_cached__gqa_block_cached__near_ridge'

def setup_inputs():

    _w = (lambda a: (lambda t: t.to(torch.bfloat16) if t.is_floating_point() else t)(torch.as_tensor(a)))
    def _A_setup():
        B, Sq, D = 16, 128, 512
        H, Sk, Dh = 32, 512, 128
        return {
            "x": _w((lambda *s: torch.randn(*s, dtype=torch.bfloat16) * 0.8)(B, Sq, D)),
            "k_cache": _w((lambda *s: torch.randn(*s, dtype=torch.bfloat16) * 0.8)(B, H, Sk, Dh)),
            "v_cache": _w((lambda *s: torch.randn(*s, dtype=torch.bfloat16) * 0.05)(B, H, Sk, Dh)),
            "wq": _w((lambda *s: torch.randn(*s, dtype=torch.bfloat16) * 0.8)(D, H * Dh) * (D ** -0.5)),
            "wo": _w((lambda *s: torch.randn(*s, dtype=torch.bfloat16) * 0.05)(H * Dh, D) * ((H * Dh) ** -0.5)),
        }
    _A_in = _A_setup()

    def _B_setup():
        B, Sq, D = 16, 128, 512
        Hq, Hkv, Sk, Dh = 32, 32, 512, 128
        assert Hq % Hkv == 0, (Hq, Hkv)
        return {
            "x": _w((lambda *s: torch.randn(*s, dtype=torch.bfloat16) * 0.8)(B, Sq, D)),
            "k_cache": _w((lambda *s: torch.randn(*s, dtype=torch.bfloat16) * 0.8)(B, Hkv, Sk, Dh)),
            "v_cache": _w((lambda *s: torch.randn(*s, dtype=torch.bfloat16) * 0.05)(B, Hkv, Sk, Dh)),
            "wq": _w((lambda *s: torch.randn(*s, dtype=torch.bfloat16) * 0.8)(D, Hq * Dh) * (D ** -0.5)),
            "wo": _w((lambda *s: torch.randn(*s, dtype=torch.bfloat16) * 0.05)(Hq * Dh, D) * ((Hq * Dh) ** -0.5)),
        }
    _B_in = _B_setup()

    return {
        "A_x": _A_in["x"],
        "A_k_cache": _A_in["k_cache"],
        "A_v_cache": _A_in["v_cache"],
        "A_wq": _A_in["wq"],
        "A_wo": _A_in["wo"],
        "B_x": _B_in["x"],
        "B_k_cache": _B_in["k_cache"],
        "B_v_cache": _B_in["v_cache"],
        "B_wq": _B_in["wq"],
        "B_wo": _B_in["wo"],
    }

def reference(A_x, A_k_cache, A_v_cache, A_wq, A_wo, B_x, B_k_cache, B_v_cache, B_wq, B_wo):
    def _A_ref(x, k_cache, v_cache, wq, wo):
        B, Sq, D = x.shape
        H, Dh = 32, 128
        q = (lambda x, *d: x.permute(*d))((x @ wq).reshape(B, Sq, H, Dh), 0, 2, 1, 3)
        scores = torch.einsum("bhqd,bhkd->bhqk", q, k_cache) * (Dh ** -0.5)
        p = _softmax(scores, axis=-1)
        o = torch.einsum("bhqk,bhkd->bhqd", p, v_cache)
        return (lambda x, *d: x.permute(*d))(o, 0, 2, 1, 3).reshape(B, Sq, H * Dh) @ wo

    def _B_ref(x, k_cache, v_cache, wq, wo):
        B, Sq, D = x.shape
        Hq, Dh = 32, 128
        Hkv = k_cache.shape[1]
        rep = Hq // Hkv
        q = (lambda x, *d: x.permute(*d))((x @ wq).reshape(B, Sq, Hq, Dh), 0, 2, 1, 3)
        k = (lambda x, n, axis: torch.repeat_interleave(x, n, dim=axis))(k_cache, rep, axis=1)
        v = (lambda x, n, axis: torch.repeat_interleave(x, n, dim=axis))(v_cache, rep, axis=1)
        scores = torch.einsum("bhqd,bhkd->bhqk", q, k) * (Dh ** -0.5)
        p = _softmax(scores, axis=-1)
        o = torch.einsum("bhqk,bhkd->bhqd", p, v)
        return (lambda x, *d: x.permute(*d))(o, 0, 2, 1, 3).reshape(B, Sq, Hq * Dh) @ wo

    a = _A_ref(A_x, A_k_cache, A_v_cache, A_wq, A_wo)
    b = _B_ref(B_x, B_k_cache, B_v_cache, B_wq, B_wo)
    return torch.concatenate([a, b], axis=-1)
```

```cpp
#include <hip/hip_runtime.h>
#include <hip/hip_bf16.h>

typedef short bf16x8 __attribute__((ext_vector_type(8)));
typedef short s16x4 __attribute__((ext_vector_type(4)));
typedef float f32x16 __attribute__((ext_vector_type(16)));
typedef float f32x2 __attribute__((ext_vector_type(2)));
typedef __bf16 bf16x2_t __attribute__((ext_vector_type(2)));
typedef unsigned u32x4 __attribute__((ext_vector_type(4)));
typedef unsigned u32x2 __attribute__((ext_vector_type(2)));
#define AS3 __attribute__((address_space(3)))
#define DI __device__ __forceinline__

constexpr int HALF = 8192;
constexpr int RINGB = 9 * HALF;
constexpr float C2 = 0.088388347648318440f * 1.4426950408889634f;
constexpr float THR = 6.0f;

#define MFMA(a, b, c) __builtin_amdgcn_mfma_f32_32x32x16_bf16((a), (b), (c), 0, 0, 0)
#define LBAR() asm volatile("s_waitcnt lgkmcnt(0)\n\ts_barrier" ::: "memory")
#define SB() __builtin_amdgcn_sched_barrier(0)
#define PIN(x) asm volatile("" : "+v"(x))
#define GAPEND(acc) do { asm volatile("" : "+v"(acc) :: "memory"); SB(); } while (0)
#define MX3(a, b, c) __builtin_fmaxf(__builtin_fmaxf((a), (b)), (c))

DI unsigned cvtpk(float lo, float hi) {
    f32x2 v = {lo, hi};
    bf16x2_t b = __builtin_convertvector(v, bf16x2_t);
    return __builtin_bit_cast(unsigned, b);
}
template <int S> DI bf16x8 pack8(const f32x16& x) {
    u32x4 p = {cvtpk(x[8 * S + 0], x[8 * S + 1]), cvtpk(x[8 * S + 2], x[8 * S + 3]),
               cvtpk(x[8 * S + 4], x[8 * S + 5]), cvtpk(x[8 * S + 6], x[8 * S + 7])};
    return __builtin_bit_cast(bf16x8, p);
}
DI bf16x8 lds128(unsigned a) { return *(const AS3 bf16x8*)(size_t)(a); }
DI s16x4 ldstr(unsigned a) {
    return __builtin_bit_cast(s16x4, __builtin_amdgcn_ds_read_tr16_b64_v4i16((AS3 s16x4*)(size_t)(a)));
}
DI bf16x8 cat(s16x4 lo, s16x4 hi) { return __builtin_shufflevector(lo, hi, 0, 1, 2, 3, 4, 5, 6, 7); }
DI bf16x8 ldtr2(unsigned a0, unsigned a1) { return cat(ldstr(a0), ldstr(a1)); }
DI float swapmax(float x) {
    auto r = __builtin_amdgcn_permlane32_swap(__float_as_uint(x), __float_as_uint(x), false, false);
    return __builtin_fmaxf(__uint_as_float(r[0]), __uint_as_float(r[1]));
}
DI float swapsum(float x) {
    auto r = __builtin_amdgcn_permlane32_swap(__float_as_uint(x), __float_as_uint(x), false, false);
    return __uint_as_float(r[0]) + __uint_as_float(r[1]);
}

DI void dma2_nt(const char* sb, unsigned voff, unsigned m) {
    asm volatile(
        "s_mov_b32 m0, %2\n\t"
        "s_nop 0\n\t"
        "global_load_lds_dwordx4 %0, %1 nt\n\t"
        "s_add_u32 m0, %2, 896\n\t"
        "s_nop 0\n\t"
        "global_load_lds_dwordx4 %0, %1 offset:128 nt"
        :: "v"(voff), "s"(sb), "s"(m) : "memory", "scc");
}
DI void dma2(const char* sb, unsigned voff, unsigned m) {
    asm volatile(
        "s_mov_b32 m0, %2\n\t"
        "s_nop 0\n\t"
        "global_load_lds_dwordx4 %0, %1\n\t"
        "s_add_u32 m0, %2, 896\n\t"
        "s_nop 0\n\t"
        "global_load_lds_dwordx4 %0, %1 offset:128"
        :: "v"(voff), "s"(sb), "s"(m) : "memory", "scc");
}
DI void dma_x(const char* sb0, const char* sb1, unsigned voff, unsigned m) {
    asm volatile(
        "s_mov_b32 m0, %3\n\t"
        "s_nop 0\n\t"
        "global_load_lds_dwordx4 %0, %1\n\t"
        "s_add_u32 m0, %3, 1024\n\t"
        "s_nop 0\n\t"
        "global_load_lds_dwordx4 %0, %2"
        :: "v"(voff), "s"(sb0), "s"(sb1), "s"(m) : "memory", "scc");
}

#define ARRIVE(N) do { unsigned long long sv_; asm volatile( \
        "s_waitcnt vmcnt(" #N ") lgkmcnt(0)\n\t" \
        "s_mov_b64 %0, exec\n\t" \
        "s_mov_b64 exec, 1\n\t" \
        "ds_add_u32 %1, %2\n\t" \
        "s_mov_b64 exec, %0" : "=&s"(sv_) : "v"(cnt), "v"(one) : "memory"); arr += 4; } while (0)
DI void wait_cnt(unsigned cnt, unsigned target) {
    unsigned v, s;
    asm volatile(
        "1:\n\t"
        "ds_read_b32 %0, %2\n\t"
        "s_waitcnt lgkmcnt(0)\n\t"
        "v_readfirstlane_b32 %1, %0\n\t"
        "s_nop 0\n\t"
        "s_cmp_ge_u32 %1, %3\n\t"
        "s_cbranch_scc0 1b"
        : "=&v"(v), "=&s"(s) : "v"(cnt), "s"(target) : "memory", "scc");
}
#define POLL() (*(volatile AS3 unsigned*)(size_t)cnt)
#define CHECK(pv) do { if (__builtin_expect((unsigned)__builtin_amdgcn_readfirstlane(pv) < arr, 0)) wait_cnt(cnt, arr); \
                       asm volatile("" ::: "memory"); SB(); } while (0)

#define STAMP(i)

DI unsigned nexth(unsigned s) { return s == 8u * HALF ? 0u : s + HALF; }

__global__ __launch_bounds__(512, 2) void fused_attn(const char* __restrict__ xA, const char* __restrict__ kA,
                                                     const char* __restrict__ vA, const char* __restrict__ wqA,
                                                     const char* __restrict__ woA, const char* __restrict__ xB,
                                                     const char* __restrict__ kB, const char* __restrict__ vB,
                                                     const char* __restrict__ wqB, const char* __restrict__ woB,
                                                     char* __restrict__ part) {
    __shared__ __attribute__((aligned(1024))) char lds[2 * RINGB + 256];
    const int tid = threadIdx.x, lane = tid & 63;
    const int wave = __builtin_amdgcn_readfirstlane(tid >> 6);
    const int g = wave >> 2, wr = wave & 3;
    const int bid = blockIdx.x;
#define MAPV 0
    const int hg = bid & 7, idx = bid >> 3;
    const int blk = idx >> 4, b = idx & 15;

    const char* X = (blk ? xB : xA) + (size_t)b * 131072 + wr * 32768;
    const char* WQ = (blk ? wqB : wqA) + (size_t)wr * 65536 + (4 * hg + g) * 256;
    const char* KK = (blk ? kB : kA) + ((size_t)b * 32 + 4 * hg + g) * 131072 + wr * 2048;
    const char* VV = (blk ? vB : vA) + ((size_t)b * 32 + 4 * hg + g) * 131072 + wr * 2048;
    const char* WO = (blk ? woB : woA) + (size_t)(4 * hg) * 131072 + wr * 8192 + g * 512;

    const unsigned lds0 = (unsigned)(size_t)(AS3 char*)lds;
    const unsigned ring = lds0 + g * RINGB;
    const unsigned mbase = ring + wr * 2048;
    const unsigned cnt = lds0 + 2 * RINGB + g * 128;
    const unsigned one = 1;
    if (tid < 64) *(AS3 unsigned*)(size_t)(lds0 + 2 * RINGB + tid * 4) = 0;

    const int r32 = lane & 31, h = lane >> 5;
    const unsigned colb = (unsigned)(h * 64 + 16 * ((lane & 3) ^ ((2 * wr + ((lane >> 4) & 1)) & 3)));
    const unsigned rowl = (lane >> 2) & 7;
    const unsigned voffKV = rowl * 256 + colb, voffWQ = rowl * 8192 + colb, voffWO = rowl * 1024 + colb;
    const unsigned voffX = (lane >> 2) * 1024 + 16 * ((lane & 3) ^ ((lane >> 4) & 3));
    const unsigned blkb = (lane >> 4) & 1, q4 = (lane & 15) >> 2, p4 = lane & 3;
    const unsigned xr = (r32 >> 2) & 3;
    const unsigned rdK = ring + 2048 * (r32 >> 3) + 64 * (r32 & 7) + 16 * (h ^ xr);
    const unsigned rdV = ring + 64 * (4 * h + q4) + 16 * ((2 * blkb + (p4 >> 1)) ^ h) + 8 * (p4 & 1);
    const unsigned rdW = ring + 2048 * h + 64 * q4 + 16 * ((2 * blkb + (p4 & 1)) ^ (2 * h)) + 8 * (p4 >> 1);
    const unsigned rdX = ring + wr * 2048 + 64 * r32 + 16 * (h ^ xr);

    unsigned cs = 0, ps = 0;
    unsigned arr = 0;
    STAMP(0);

#define PSLOT() (pm_ = mbase + ps, ps = nexth(ps), pm_)
    unsigned pm_;
#define QI(i) (i)
#define XI(i) QI(i)
#define IS_X(i)   do { const char* s_ = X + XI(i) * 64; dma_x(s_, s_ + 16384, voffX, PSLOT()); } while (0)
#define IS_WA(i)  dma2(WQ + (size_t)QI(i) * 262144, voffWQ, PSLOT())
#define IS_WB(i)  dma2(WQ + (size_t)QI(i) * 262144 + 512, voffWQ, PSLOT())
#define IS_K(n, hf) dma2_nt(KK + ((n) >> 3) * 262144 + ((n) & 7) * 16384 + (hf) * 8192, voffKV, PSLOT())
#define IS_V(n, hf) dma2_nt(VV + ((n) >> 3) * 262144 + ((n) & 7) * 16384 + (hf) * 8192, voffKV, PSLOT())
#define IS_WO(z) do { const int kt_ = ((z) >> 1) & 7, hh_ = 2 * ((kt_ >> 1) & 1) + ((kt_ >> 2) ? 1 - g : g); \
        dma2(WO + (size_t)hh_ * 131072 + ((kt_ & 1) * 2 + ((z) & 1)) * 32768 + ((z) >> 4) * 256, voffWO, PSLOT()); } while (0)

    bf16x8 F0[4], F1[4], xf0, xf1;

    IS_X(0); IS_WA(0); IS_WB(0); IS_X(1); IS_WA(1); IS_WB(1); IS_X(2); IS_WA(2); IS_WB(2);
    LBAR();
    ARRIVE(12);
    wait_cnt(cnt, arr);
    {
        const unsigned aw = rdW + HALF;
        xf0 = lds128(rdX);
#pragma unroll
        for (int j = 0; j < 4; ++j) F0[j] = ldtr2(aw + 512 * j, (aw ^ 16) + 512 * j + 256);
    }

    bf16x8 qf[8], sh[8];
    {
        f32x16 qa[4] = {}, qb[4] = {};
#pragma unroll 1
        for (int u = 0; u < 16; ++u) {
            const unsigned c0 = cs, c1 = nexth(c0), c2 = nexth(c1); cs = nexth(c2);
            const unsigned wa = rdW + c1, wa1 = wa ^ 16, wb = rdW + c2, wb1 = wb ^ 16;
            xf1 = lds128((rdX + c0) ^ 32);
#pragma unroll
            for (int j = 0; j < 4; ++j) {
                F1[j] = ldtr2(wa + 4096 + 512 * j, wa1 + 4096 + 512 * j + 256);
                qa[j] = MFMA(F0[j], xf0, qa[j]);
                if (j == 1 && u > 0) { if (u <= 13) IS_X(u + 2); else if (u == 14) IS_K(0, 0); else IS_V(0, 1); }
                GAPEND(qa[j]);
            }
#pragma unroll
            for (int j = 0; j < 4; ++j) {
                F0[j] = ldtr2(wb + 512 * j, wb1 + 512 * j + 256);
                qa[j] = MFMA(F1[j], xf1, qa[j]);
                if (j == 0 && u > 0) { if (u <= 13) IS_WA(u + 2); else if (u == 14) IS_K(0, 1); else IS_K(1, 0); }
                if (j == 2 && u > 0) { if (u <= 13) IS_WB(u + 2); else if (u == 14) IS_V(0, 0); else IS_K(1, 1); }
                GAPEND(qa[j]);
            }
#pragma unroll
            for (int j = 0; j < 4; ++j) {
                if (j < 2) {
                    F1[2 * j] = ldtr2(wb + 4096 + 512 * (2 * j), wb1 + 4096 + 512 * (2 * j) + 256);
                    F1[2 * j + 1] = ldtr2(wb + 4096 + 512 * (2 * j + 1), wb1 + 4096 + 512 * (2 * j + 1) + 256);
                }
                qb[j] = MFMA(F0[j], xf0, qb[j]);
                GAPEND(qb[j]);
            }
            ARRIVE(6);
            const unsigned pv = POLL();
            qb[0] = MFMA(F1[0], xf1, qb[0]); GAPEND(qb[0]);
            qb[1] = MFMA(F1[1], xf1, qb[1]); GAPEND(qb[1]);
            CHECK(pv);
            {
                const unsigned n0 = cs, n1 = nexth(cs);
                if (u < 15) {
                    const unsigned an = rdW + n1;
                    xf0 = lds128(rdX + n0);
#pragma unroll
                    for (int j = 0; j < 4; ++j) F0[j] = ldtr2(an + 512 * j, (an ^ 16) + 512 * j + 256);
                } else {
                    const unsigned k0 = rdK + n0;
                    F0[0] = lds128(k0); F0[1] = lds128(k0 ^ 32); F0[2] = lds128(k0 + 512); F0[3] = lds128((k0 ^ 32) + 512);
                }
            }
            qb[2] = MFMA(F1[2], xf1, qb[2]); GAPEND(qb[2]);
            qb[3] = MFMA(F1[3], xf1, qb[3]); GAPEND(qb[3]);
        }
#pragma unroll
        for (int dt = 0; dt < 4; ++dt) {
            qf[2 * dt] = pack8<0>(qa[dt]); qf[2 * dt + 1] = pack8<1>(qa[dt]);
            sh[2 * dt] = pack8<0>(qb[dt]); sh[2 * dt + 1] = pack8<1>(qb[dt]);
        }
    }
    STAMP(1);

    f32x16 o[4] = {};
    float m = -1e30f, l = 0.f;
#pragma unroll 1
    for (int tt = 0; tt < 16; ++tt) {
        f32x16 s0 = {}, s1 = {};
        float pm;
        {
            const unsigned c0 = cs, c1 = nexth(c0); cs = nexth(c1);
            const unsigned kl = rdK + c0, klo = kl ^ 32, kh = rdK + c1, kho = kh ^ 32;
            const unsigned pv = POLL();
#define KRD(e, o_, d) (((d) & 1) ? lds128((o_) + 512 * ((d) >> 1)) : lds128((e) + 512 * ((d) >> 1)))
#pragma unroll
            for (int j = 0; j < 4; ++j) { F1[j] = KRD(kl, klo, 4 + j); s0 = MFMA(F0[j], qf[j], s0); GAPEND(s0); }
            CHECK(pv);
#pragma unroll
            for (int j = 0; j < 4; ++j) {
                F0[j] = KRD(kh, kho, j); s0 = MFMA(F1[j], qf[4 + j], s0);
                if (j == 0 && tt == 0) IS_V(1, 0);
                if (j == 1 && tt < 15) IS_V(tt + 1, 1);
                if (j == 3 && tt < 15) { if (tt < 14) IS_K(tt + 2, 0); else IS_WO(0); }
                GAPEND(s0);
            }
#pragma unroll
            for (int j = 0; j < 4; ++j) {
                if (j < 2) { F1[2 * j] = KRD(kh, kho, 4 + 2 * j); F1[2 * j + 1] = KRD(kh, kho, 5 + 2 * j); }
                s1 = MFMA(F0[j], qf[j], s1);
                if (j == 1) { pm = MX3(s0[0], s0[1], s0[2]); pm = MX3(pm, s0[3], s0[4]); pm = MX3(pm, s0[5], s0[6]); PIN(pm); }
                if (j == 2) { pm = MX3(pm, s0[7], s0[8]); pm = MX3(pm, s0[9], s0[10]); pm = MX3(pm, s0[11], s0[12]); PIN(pm); }
                if (j == 3) { pm = MX3(pm, s0[13], s0[14]); pm = __builtin_fmaxf(pm, s0[15]); PIN(pm); }
                GAPEND(s1);
            }
            if (tt == 15) ARRIVE(2); else ARRIVE(8);
            const unsigned a0 = rdV + cs, a1 = a0 ^ 32;
#pragma unroll
            for (int j = 0; j < 4; ++j) {
                F0[j] = ldtr2(a0 + 512 * j, a1 + 2048 + 512 * j);
                s1 = MFMA(F1[j], qf[4 + j], s1); GAPEND(s1);
            }
        }
        {
            float pb = MX3(s1[0], s1[1], s1[2]);
            pm = MX3(pm, s1[3], s1[4]); pb = MX3(pb, s1[5], s1[6]);
            pm = MX3(pm, s1[7], s1[8]); pb = MX3(pb, s1[9], s1[10]);
            pm = MX3(pm, s1[11], s1[12]); pb = MX3(pb, s1[13], s1[14]);
            pm = MX3(pm, pb, s1[15]);
            pm = swapmax(pm);
            if (__builtin_expect(__any((pm - m) * C2 > THR), 0)) {
                const float mn = __builtin_fmaxf(m, pm);
                const float al = __builtin_amdgcn_exp2f((m - mn) * C2);
                m = mn; l *= al;
#pragma unroll
                for (int dt = 0; dt < 4; ++dt)
#pragma unroll
                    for (int i = 0; i < 16; ++i) o[dt][i] *= al;
            }
        }
        const float nm = -m * C2;
#define EX(v) __builtin_amdgcn_exp2f(__builtin_fmaf((v), C2, nm))
        u32x4 pw0, pw1, pw2, pw3;
#pragma unroll
        for (int i = 0; i < 8; ++i) s0[i] = EX(s0[i]);
#pragma unroll
        for (int i = 0; i < 4; ++i) pw0[i] = cvtpk(s0[2 * i], s0[2 * i + 1]);
        PIN(pw0); SB();
        {
            const unsigned c0 = cs, c1 = nexth(c0); cs = nexth(c1);
            const unsigned l0 = rdV + c0, l1 = l0 ^ 32, h0 = rdV + c1, h1 = h0 ^ 32;
            const unsigned pv = POLL();
            float ls = 0.f;
#define PF(w) __builtin_bit_cast(bf16x8, w)
#define FILL(S, B, PW, S2, B2, j) do { S[(B) + 2 * (j)] = EX(S[(B) + 2 * (j)]); S[(B) + 2 * (j) + 1] = EX(S[(B) + 2 * (j) + 1]); \
                PW[j] = cvtpk(S[(B) + 2 * (j)], S[(B) + 2 * (j) + 1]); ls += S2[(B2) + 2 * (j)]; ls += S2[(B2) + 2 * (j) + 1]; \
                PIN(PW); PIN(ls); PIN(S); } while (0)
#pragma unroll
            for (int j = 0; j < 4; ++j) {
                F1[j] = ldtr2(l0 + 4096 + 512 * j, l1 + 4096 + 2048 + 512 * j);
                o[j] = MFMA(F0[j], PF(pw0), o[j]);
                FILL(s0, 8, pw1, s0, 0, j);
                GAPEND(o[j]);
            }
            CHECK(pv);
#pragma unroll
            for (int j = 0; j < 4; ++j) {
                F0[j] = ldtr2(h0 + 512 * j, h1 + 2048 + 512 * j);
                o[j] = MFMA(F1[j], PF(pw1), o[j]);
                FILL(s1, 0, pw2, s0, 8, j);
                if (j == 1 && tt < 14) IS_K(tt + 2, 1);
                if (j == 3 && tt < 14) IS_V(tt + 2, 0);
                GAPEND(o[j]);
            }
#pragma unroll
            for (int j = 0; j < 4; ++j) {
                if (j < 2) {
                    F1[2 * j] = ldtr2(h0 + 4096 + 512 * (2 * j), h1 + 4096 + 2048 + 512 * (2 * j));
                    F1[2 * j + 1] = ldtr2(h0 + 4096 + 512 * (2 * j + 1), h1 + 4096 + 2048 + 512 * (2 * j + 1));
                }
                o[j] = MFMA(F0[j], PF(pw2), o[j]);
                FILL(s1, 8, pw3, s1, 0, j);
                GAPEND(o[j]);
            }
            if (tt >= 14) ARRIVE(4); else ARRIVE(8);
            const unsigned k0 = rdK + cs, k0o = k0 ^ 32;
#pragma unroll
            for (int j = 0; j < 4; ++j) {
                if (tt < 15) F0[j] = KRD(k0, k0o, j);
                o[j] = MFMA(F1[j], PF(pw3), o[j]);
                ls += s1[8 + 2 * j]; ls += s1[9 + 2 * j]; PIN(ls);
                GAPEND(o[j]);
            }
            l += ls;
        }
        if (tt == 7) {
            const float inv = __builtin_amdgcn_rcpf(swapsum(l));
#pragma unroll
            for (int dt = 0; dt < 4; ++dt) {
#pragma unroll
                for (int i = 0; i < 16; ++i) o[dt][i] *= inv;
                qf[2 * dt] = sh[2 * dt]; qf[2 * dt + 1] = sh[2 * dt + 1];
                sh[2 * dt] = pack8<0>(o[dt]); sh[2 * dt + 1] = pack8<1>(o[dt]);
                o[dt] = f32x16{};
            }
            m = -1e30f; l = 0.f;
            STAMP(2);
        }
    }
    STAMP(4);
    bf16x8 st1[8];
    {
        const float inv = __builtin_amdgcn_rcpf(swapsum(l));
#pragma unroll
        for (int dt = 0; dt < 4; ++dt) {
#pragma unroll
            for (int i = 0; i < 16; ++i) o[dt][i] *= inv;
            st1[2 * dt] = pack8<0>(o[dt]); st1[2 * dt + 1] = pack8<1>(o[dt]);
        }
    }

    bf16x8 pt0[8], pt1[8];
    {
        wait_cnt(cnt, arr);
        const unsigned e0 = (wr < 2 ? 5u + 2 * wr : 2u * wr - 4) * HALF + lane * 16, e1 = e0 + HALF;
#pragma unroll
        for (int f = 0; f < 8; ++f) {
            *(AS3 bf16x8*)(size_t)(ring + e0 + f * 1024) = sh[f];
            *(AS3 bf16x8*)(size_t)(ring + e1 + f * 1024) = st1[f];
        }
        LBAR();
        const unsigned oring = lds0 + (1 - g) * RINGB;
#pragma unroll
        for (int f = 0; f < 8; ++f) { pt0[f] = lds128(oring + e0 + f * 1024); pt1[f] = lds128(oring + e1 + f * 1024); }
        LBAR();
#pragma unroll
        for (int z = 1; z < 9; ++z) IS_WO(z);
        ARRIVE(12);
        wait_cnt(cnt, arr);
        const unsigned a0 = rdV + cs, a1 = a0 ^ 32;
#pragma unroll
        for (int j = 0; j < 4; ++j) F0[j] = ldtr2(a0 + 512 * j, a1 + 2048 + 512 * j);
    }

    STAMP(5);
    char* pw = part + ((size_t)(idx * 8 + hg) * 8 + wave) * 16384 + lane * 16;
#pragma unroll
    for (int c = 0; c < 2; ++c) {
        f32x16 oa[4] = {};
#pragma unroll
        for (int kt = 0; kt < 8; ++kt) {
            const int o_ = c * 8 + kt;
            const unsigned c0 = cs, c1 = nexth(c0); cs = nexth(c1);
            const unsigned l0 = rdV + c0, l1 = l0 ^ 32, h0 = rdV + c1, h1 = h0 ^ 32;
#define OFR(ks) ((kt >> 1) == 0 ? sh[(kt & 1) * 4 + (ks)] : (kt >> 1) == 1 ? st1[(kt & 1) * 4 + (ks)] : (kt >> 1) == 2 ? pt0[(kt & 1) * 4 + (ks)] : pt1[(kt & 1) * 4 + (ks)])
            const unsigned pv = POLL();
#pragma unroll
            for (int j = 0; j < 4; ++j) {
                F1[j] = ldtr2(l0 + 4096 + 512 * j, l1 + 4096 + 2048 + 512 * j);
                oa[j] = MFMA(F0[j], OFR(0), oa[j]); GAPEND(oa[j]);
            }
            CHECK(pv);
#pragma unroll
            for (int j = 0; j < 4; ++j) {
                F0[j] = ldtr2(h0 + 512 * j, h1 + 2048 + 512 * j);
                oa[j] = MFMA(F1[j], OFR(1), oa[j]);
                if (j == 1 && o_ >= 1 && 7 + 2 * o_ < 32) IS_WO(7 + 2 * o_);
                if (j == 3 && o_ >= 1 && 8 + 2 * o_ < 32) IS_WO(8 + 2 * o_);
                GAPEND(oa[j]);
            }
#pragma unroll
            for (int j = 0; j < 4; ++j) {
                if (j < 2) {
                    F1[2 * j] = ldtr2(h0 + 4096 + 512 * (2 * j), h1 + 4096 + 2048 + 512 * (2 * j));
                    F1[2 * j + 1] = ldtr2(h0 + 4096 + 512 * (2 * j + 1), h1 + 4096 + 2048 + 512 * (2 * j + 1));
                }
                oa[j] = MFMA(F0[j], OFR(2), oa[j]); GAPEND(oa[j]);
            }
            if (o_ == 8) ARRIVE(16);
            else if (o_ <= 11) ARRIVE(8);
            else if (o_ == 12) ARRIVE(6);
            else if (o_ == 13) ARRIVE(2);
            else if (o_ == 14) ARRIVE(0);
            const unsigned a0 = rdV + cs, a1 = a0 ^ 32;
#pragma unroll
            for (int j = 0; j < 4; ++j) {
                if (o_ < 15) F0[j] = ldtr2(a0 + 512 * j, a1 + 2048 + 512 * j);
                oa[j] = MFMA(F1[j], OFR(3), oa[j]); GAPEND(oa[j]);
            }
            if (kt == 7) {
#pragma unroll
                for (int nt = 0; nt < 4; ++nt) {
                    *(bf16x8*)(pw + ((c * 4 + nt) * 2 + 0) * 1024) = pack8<0>(oa[nt]);
                    *(bf16x8*)(pw + ((c * 4 + nt) * 2 + 1) * 1024) = pack8<1>(oa[nt]);
                }
                asm volatile("" ::: "memory"); SB();
            }
        }
        STAMP(6 + c);
    }
}

__global__ __launch_bounds__(256) void reduce_out(const char* __restrict__ part, unsigned short* __restrict__ out) {
    const int gid = blockIdx.x * 256 + threadIdx.x;
    const int lane = gid & 63, fr = (gid >> 6) & 15, wave = (gid >> 10) & 7, idx = gid >> 13;
    float acc[8] = {};
#pragma unroll
    for (int hg = 0; hg < 8; ++hg) {
        const int bid = idx * 8 + hg;
        const u32x4 v = *(const u32x4*)(part + ((size_t)bid * 8 + wave) * 16384 + fr * 1024 + lane * 16);
#pragma unroll
        for (int j = 0; j < 4; ++j) {
            acc[2 * j] += __uint_as_float(v[j] << 16);
            acc[2 * j + 1] += __uint_as_float(v[j] & 0xffff0000u);
        }
    }
    const int blk = idx >> 4, b = idx & 15, g = wave >> 2, wr = wave & 3;
    const int c = fr >> 3, nt = (fr >> 1) & 3, s = fr & 1, h = lane >> 5;
    const int row = wr * 32 + (lane & 31);
    const int n0 = g * 256 + c * 128 + nt * 32 + s * 16 + 4 * h;
    unsigned short* o = out + ((size_t)(b * 128 + row)) * 1024 + blk * 512 + n0;
    u32x2 w0 = {cvtpk(acc[0], acc[1]), cvtpk(acc[2], acc[3])};
    u32x2 w1 = {cvtpk(acc[4], acc[5]), cvtpk(acc[6], acc[7])};
    *(u32x2*)(o) = w0;
    *(u32x2*)(o + 8) = w1;
}

extern "C" void kernel_launch(void* const* d_in, const int* in_sizes, int n_in, void* d_out, int out_size, void* d_ws,
                              size_t ws_size, hipStream_t stream) {
    char* part = (char*)d_ws;
    fused_attn<<<256, 512, 0, stream>>>((const char*)d_in[0], (const char*)d_in[1], (const char*)d_in[2],
                                        (const char*)d_in[3], (const char*)d_in[4], (const char*)d_in[5],
                                        (const char*)d_in[6], (const char*)d_in[7], (const char*)d_in[8],
                                        (const char*)d_in[9], part);
    reduce_out<<<1024, 256, 0, stream>>>(part, (unsigned short*)d_out);
}
```

```cpp
#include <hip/hip_runtime.h>
#include <hip/hip_bf16.h>

typedef short bf16x8 __attribute__((ext_vector_type(8)));
typedef short s16x4 __attribute__((ext_vector_type(4)));
typedef float f32x16 __attribute__((ext_vector_type(16)));
typedef float f32x2 __attribute__((ext_vector_type(2)));
typedef __bf16 bf16x2_t __attribute__((ext_vector_type(2)));
typedef unsigned u32x4 __attribute__((ext_vector_type(4)));
typedef unsigned u32x2 __attribute__((ext_vector_type(2)));
#define AS3 __attribute__((address_space(3)))
#define DI __device__ __forceinline__

constexpr int HALF = 8192;
constexpr int RINGB = 9 * HALF;
constexpr float C2 = 0.088388347648318440f * 1.4426950408889634f;
#define PBOUND 1048576.0f

#define MFMA(a, b, c) __builtin_amdgcn_mfma_f32_32x32x16_bf16((a), (b), (c), 0, 0, 0)
#define LBAR() asm volatile("s_waitcnt lgkmcnt(0)\n\ts_barrier" ::: "memory")
#define SB() __builtin_amdgcn_sched_barrier(0)
#define PIN(x) asm volatile("" : "+v"(x))
#define GAPEND(acc) do { asm volatile("" : "+v"(acc) :: "memory"); SB(); } while (0)
#define MX3(a, b, c) __builtin_fmaxf(__builtin_fmaxf((a), (b)), (c))
#define EXP2(x) __builtin_amdgcn_exp2f(x)

DI unsigned cvtpk(float lo, float hi) {
    f32x2 v = {lo, hi};
    bf16x2_t b = __builtin_convertvector(v, bf16x2_t);
    return __builtin_bit_cast(unsigned, b);
}
template <int S> DI bf16x8 pack8(const f32x16& x) {
    u32x4 p = {cvtpk(x[8 * S + 0], x[8 * S + 1]), cvtpk(x[8 * S + 2], x[8 * S + 3]),
               cvtpk(x[8 * S + 4], x[8 * S + 5]), cvtpk(x[8 * S + 6], x[8 * S + 7])};
    return __builtin_bit_cast(bf16x8, p);
}
DI bf16x8 lds128(unsigned a) { return *(const AS3 bf16x8*)(size_t)(a); }
DI s16x4 ldstr(unsigned a) {
    return __builtin_bit_cast(s16x4, __builtin_amdgcn_ds_read_tr16_b64_v4i16((AS3 s16x4*)(size_t)(a)));
}
DI bf16x8 cat(s16x4 lo, s16x4 hi) { return __builtin_shufflevector(lo, hi, 0, 1, 2, 3, 4, 5, 6, 7); }
DI bf16x8 ldtr2(unsigned a0, unsigned a1) { return cat(ldstr(a0), ldstr(a1)); }
DI float swapmax(float x) {
    auto r = __builtin_amdgcn_permlane32_swap(__float_as_uint(x), __float_as_uint(x), false, false);
    return __builtin_fmaxf(__uint_as_float(r[0]), __uint_as_float(r[1]));
}
DI float swapsum(float x) {
    auto r = __builtin_amdgcn_permlane32_swap(__float_as_uint(x), __float_as_uint(x), false, false);
    return __uint_as_float(r[0]) + __uint_as_float(r[1]);
}
DI float max16(const f32x16& s) {
    float a = MX3(s[0], s[1], s[2]), b = MX3(s[3], s[4], s[5]);
    a = MX3(a, s[6], s[7]); b = MX3(b, s[8], s[9]);
    a = MX3(a, s[10], s[11]); b = MX3(b, s[12], s[13]);
    a = MX3(a, s[14], s[15]);
    return __builtin_fmaxf(a, b);
}
DI unsigned lane_id() { return __builtin_amdgcn_mbcnt_hi(~0u, __builtin_amdgcn_mbcnt_lo(~0u, 0u)); }

#define M0SET(base, off) asm volatile("s_add_u32 m0, %0, %1" :: "s"(base), "i"(off) : "scc")
#define GLD(voff, sb, imm) asm volatile("global_load_lds_dwordx4 %0, %1 offset:%2" :: "v"(voff), "s"(sb), "i"(imm) : "memory")
#define GLDNT(voff, sb, imm) asm volatile("global_load_lds_dwordx4 %0, %1 offset:%2 nt" :: "v"(voff), "s"(sb), "i"(imm) : "memory")
#define NOP0() asm volatile("s_nop 0")

#define ARRIVE(N) ARRIVE2(N, 0)
#define ARRIVE2(N, LG) do { unsigned long long sv_; asm volatile( \
        "s_waitcnt vmcnt(" #N ") lgkmcnt(" #LG ")\n\t" \
        "s_mov_b64 %0, exec\n\t" \
        "s_mov_b64 exec, 1\n\t" \
        "ds_add_u32 %1, %1\n\t" \
        "s_mov_b64 exec, %0" : "=&s"(sv_) : "v"(cnt) : "memory"); arr += arrinc; } while (0)
DI void wait_cnt(unsigned cnt, unsigned target) {
    unsigned v, s;
    asm volatile(
        "1:\n\t"
        "ds_read_b32 %0, %2\n\t"
        "s_waitcnt lgkmcnt(0)\n\t"
        "v_readfirstlane_b32 %1, %0\n\t"
        "s_nop 0\n\t"
        "s_cmp_ge_u32 %1, %3\n\t"
        "s_cbranch_scc0 1b"
        : "=&v"(v), "=&s"(s) : "v"(cnt), "s"(target) : "memory", "scc");
}
#define POLL() (*(volatile AS3 unsigned*)(size_t)cnt)
#define CHECK(pv) do { if (__builtin_expect((unsigned)__builtin_amdgcn_readfirstlane(pv) < arr, 0)) wait_cnt(cnt, arr); \
                       asm volatile("" ::: "memory"); SB(); } while (0)

#define STAMP(i)

__global__ __launch_bounds__(512, 2) void fused_attn(const char* __restrict__ xA, const char* __restrict__ kA,
                                                     const char* __restrict__ vA, const char* __restrict__ wqA,
                                                     const char* __restrict__ woA, const char* __restrict__ xB,
                                                     const char* __restrict__ kB, const char* __restrict__ vB,
                                                     const char* __restrict__ wqB, const char* __restrict__ woB,
                                                     char* __restrict__ part) {
    __shared__ __attribute__((aligned(1024))) char lds[2 * RINGB + 256];
    const int tid = threadIdx.x;
    const int wave = __builtin_amdgcn_readfirstlane(tid >> 6);
    const int g = wave >> 2, wr = wave & 3;
    const int bid = blockIdx.x;

    const unsigned lds0 = (unsigned)(size_t)(AS3 char*)lds;
    const unsigned ring = lds0 + g * RINGB;
    const unsigned mbase = ring + wr * 2048;
    unsigned cnt = lds0 + 2 * RINGB + g * 128;
    const unsigned arrinc = 4 * cnt;
    PIN(cnt);
    if (tid < 64) *(AS3 unsigned*)(size_t)(lds0 + 2 * RINGB + tid * 4) = 0;

    STAMP(0);
    const unsigned flagw = lds0 + 2 * RINGB + 64;
#pragma unroll 1
    for (int pass = 0; pass < 2; ++pass) {
    unsigned arr = 0;
    int bid_ = bid; asm volatile("" : "+s"(bid_));
    const int hg = bid_ & 7, idx = bid_ >> 3, blk = idx >> 4, b = idx & 15;
    const char* X = (blk ? xB : xA) + (size_t)b * 131072 + wr * 32768;
    const char* WQ = (blk ? wqB : wqA) + (size_t)wr * 65536 + (4 * hg + g) * 256;
    const char* KK = (blk ? kB : kA) + ((size_t)b * 32 + 4 * hg + g) * 131072 + wr * 2048;
    const char* VV = (blk ? vB : vA) + ((size_t)b * 32 + 4 * hg + g) * 131072 + wr * 2048;
    const char* WO = (blk ? woB : woA) + (size_t)(4 * hg) * 131072 + wr * 8192 + g * 512;

    unsigned lane = lane_id(); PIN(lane);
    const unsigned r32 = lane & 31, h = lane >> 5;
    const unsigned colb = (unsigned)(h * 64 + 16 * ((lane & 3) ^ ((2 * wr + ((lane >> 4) & 1)) & 3)));
    const unsigned rowl = (lane >> 2) & 7;
    const unsigned vWQ = rowl * 8192 + colb;
    const unsigned vX = (lane >> 2) * 1024 + 16 * ((lane & 3) ^ ((lane >> 4) & 3)), vX2 = vX + 16384;
    const unsigned blkb = (lane >> 4) & 1, q4 = (lane & 15) >> 2, p4 = lane & 3;
    const unsigned xr = (r32 >> 2) & 3;
    unsigned rdK0 = ring + 2048 * (r32 >> 3) + 64 * (r32 & 7) + 16 * (h ^ xr), rdK1 = rdK0 ^ 32;
    unsigned rdV0 = ring + 64 * (4 * h + q4) + 16 * ((2 * blkb + (p4 >> 1)) ^ h) + 8 * (p4 & 1), rdV1 = rdV0 ^ 32;
    const unsigned rdW0 = ring + 2048 * h + 64 * q4 + 16 * ((2 * blkb + (p4 & 1)) ^ (2 * h)) + 8 * (p4 >> 1), rdW1 = rdW0 ^ 16;
    const unsigned rdW0x = rdW0 + 8 * HALF, rdW1x = rdW1 + 8 * HALF;
    const unsigned rdX0 = ring + wr * 2048 + 64 * r32 + 16 * (h ^ xr), rdX1 = rdX0 ^ 32;

    unsigned vT = rowl * 256 + colb, vT2 = vT + 8192;

    bf16x8 R[4], xf0, xf1;
#define WRD(s, ks, j) ((s) == 8 ? ldtr2(rdW0x + 4096 * (ks) + 512 * (j), rdW1x + 4096 * (ks) + 512 * (j) + 256) \
                                : ldtr2(rdW0 + (s) * HALF + 4096 * (ks) + 512 * (j), rdW1 + (s) * HALF + 4096 * (ks) + 512 * (j) + 256))
#define KRD(s, d) (((d) & 1) ? lds128(rdK1 + (s) * HALF + 512 * ((d) >> 1)) : lds128(rdK0 + (s) * HALF + 512 * ((d) >> 1)))
#define VRD(s, k1, j) ldtr2(rdV0 + (s) * HALF + 4096 * (k1) + 512 * (j), rdV1 + (s) * HALF + 4096 * (k1) + 2048 + 512 * (j))

#define QFR(s, i) WRD((s) + 1 + ((i) >> 3), ((i) >> 2) & 1, (i) & 3)
#define KFR(s, i) KRD((s) + ((i) >> 3), (i) & 7)
#define VFR(s, i) VRD((s) + ((i) >> 3), ((i) >> 2) & 1, (i) & 3)

#pragma unroll
    for (int u = 0; u < 3; ++u) {
        const int s = 3 * ((u + 2) % 3);
        const char* px = X + u * 64; const char* pw = WQ + (size_t)u * 262144;
        M0SET(mbase, s * HALF); NOP0(); GLD(vX, px, 0); M0SET(mbase, s * HALF + 1024); NOP0(); GLD(vX2, px, 0);
        M0SET(mbase, (s + 1) * HALF); NOP0(); GLD(vWQ, pw, 0); M0SET(mbase, (s + 1) * HALF + 896); NOP0(); GLD(vWQ, pw, 128);
        M0SET(mbase, (s + 2) * HALF - 512); NOP0(); GLD(vWQ, pw, 512); M0SET(mbase, (s + 2) * HALF + 896 - 512); NOP0(); GLD(vWQ, pw, 640);
    }
    LBAR();
    ARRIVE(12);
    wait_cnt(cnt, arr);
    STAMP(1);
    xf0 = lds128(rdX0 + 6 * HALF);
#pragma unroll
    for (int j = 0; j < 3; ++j) R[j] = QFR(6, j);

    bf16x8 qf[8], sh[8];
    {
        f32x16 qa[4] = {}, qb[4] = {};
        const char* px = X + 3 * 64; const char* pw = WQ + (size_t)3 * 262144;
#define QM0(MODE, sp, p) do { if (MODE == 1) { \
                    M0SET(mbase, (p) == 0 ? (sp) * HALF : (p) == 1 ? (sp) * HALF + 1024 : (p) == 2 ? ((sp) + 1) * HALF : (p) == 3 ? ((sp) + 1) * HALF + 896 \
                          : (p) == 4 ? ((sp) + 2) * HALF - 512 : ((sp) + 2) * HALF + 896 - 512); \
                } else if (MODE >= 2) { M0SET(mbase, ((sp) + ((p) >> 1)) * HALF + ((p) & 1) * 896); } } while (0)
#define QLD(MODE, p) do { if (MODE == 1) { \
                    if ((p) == 0) GLD(vX, px, 0); else if ((p) == 1) GLD(vX2, px, 0); else if ((p) == 2) GLD(vWQ, pw, 0); \
                    else if ((p) == 3) GLD(vWQ, pw, 128); else if ((p) == 4) GLD(vWQ, pw, 512); else GLD(vWQ, pw, 640); \
                } else if (MODE == 2) { \
                    if ((p) < 2) GLDNT(vT, KK, ((p) & 1) * 128); else if ((p) < 4) GLDNT(vT2, KK, ((p) & 1) * 128); else GLDNT(vT, VV, ((p) & 1) * 128); \
                } else if (MODE == 3) { \
                    const char* k1_ = KK + 16384; \
                    if ((p) < 2) GLDNT(vT2, VV, ((p) & 1) * 128); else if ((p) < 4) GLDNT(vT, k1_, ((p) & 1) * 128); else GLDNT(vT2, k1_, ((p) & 1) * 128); \
                } } while (0)
#define QP(acc, MODE, p) do { if (MODE) { PIN(acc); QLD(MODE, p); } } while (0)
#define QSTEP(U3, MODE) do { \
            constexpr int s = 3 * (((U3) + 2) % 3), sp = 3 * (((U3) + 1) % 3), sn = 3 * ((U3) % 3); \
            unsigned pv; \
            QM0(MODE, sp, 0); SB(); \
            xf1 = lds128(rdX1 + s * HALF); \
            _Pragma("unroll") for (int i = 0; i < 16; ++i) { \
                const bf16x8 cur = R[i & 3]; \
                if (i == 13) { ARRIVE(6); pv = POLL(); } \
                if (i == 15) { CHECK(pv); \
                    if (MODE != 3) { xf0 = lds128(rdX0 + sn * HALF); _Pragma("unroll") for (int j = 0; j < 3; ++j) R[j] = QFR(sn, j); } \
                    else { _Pragma("unroll") for (int j = 0; j < 3; ++j) R[j] = KFR(0, j); } } \
                if (i < 13) R[(i + 3) & 3] = QFR(s, i + 3); \
                if (i < 8) { qa[i & 3] = MFMA(cur, (i & 4) ? xf1 : xf0, qa[i & 3]); \
                    if (!(i & 1)) QP(qa[i & 3], MODE, i >> 1); else QM0(MODE, sp, (i >> 1) + 1); \
                    GAPEND(qa[i & 3]); } \
                else { qb[i & 3] = MFMA(cur, (i & 4) ? xf1 : xfk, qb[i & 3]); \
                    if (i < 12) { if (!(i & 1)) QP(qb[i & 3], MODE, i >> 1); else if (i == 9) QM0(MODE, sp, 5); } \
                    GAPEND(qb[i & 3]); } \
            } \
            if (MODE == 1) { px += 64; pw += 262144; } \
        } while (0)
#define xfk xf0
        QSTEP(0, 0);
#pragma unroll 1
        for (int i = 0; i < 4; ++i) { QSTEP(1, 1); QSTEP(2, 1); QSTEP(0, 1); }
        QSTEP(1, 1); QSTEP(2, 2); QSTEP(0, 3);
#pragma unroll
        for (int dt = 0; dt < 4; ++dt) {
#pragma unroll
            for (int i = 0; i < 16; ++i) { qa[dt][i] *= C2; qb[dt][i] *= C2; }
            qf[2 * dt] = pack8<0>(qa[dt]); qf[2 * dt + 1] = pack8<1>(qa[dt]);
            sh[2 * dt] = pack8<0>(qb[dt]); sh[2 * dt + 1] = pack8<1>(qb[dt]);
        }
    }
    STAMP(2);

    f32x16 o[4];
    f32x16 ng;
    float l = 0.f;
    const char* pV = VV + 16384;
    const char* pK = KK + 32768;
    unsigned mcur = mbase, moth = mbase + 4 * HALF;
    int dlt = 4 * HALF;
#define PF(w) __builtin_bit_cast(bf16x8, w)
#define TM0(MB, SL, j) M0SET(MB, ((SL) + ((j) >> 1)) * HALF + ((j) & 1) * 896)
#define TLD(P, j) do { if ((j) < 2) GLDNT(vT, P, ((j) & 1) * 128); else GLDNT(vT2, P, ((j) & 1) * 128); } while (0)
#define TLDV(j) do { if (tt < 15) TLD(pV, j); } while (0)
#define TLDK(j) do { if (tt < 14) TLD(pK, j); } while (0)
#define TLD_WO(P, j) do { if ((j) < 2) GLD(vT, P, ((j) & 1) * 128); else GLD(vT2, P, ((j) & 1) * 128); } while (0)
#define EXPK(S, B, PW, j) do { S[(B) + 2 * (j)] = EXP2(S[(B) + 2 * (j)]); S[(B) + 2 * (j) + 1] = EXP2(S[(B) + 2 * (j) + 1]); \
                               PW[j] = cvtpk(S[(B) + 2 * (j)], S[(B) + 2 * (j) + 1]); } while (0)
#define ADD2(S, B, j) do { ls += S[(B) + 2 * (j)]; ls += S[(B) + 2 * (j) + 1]; } while (0)
#pragma unroll 1
    for (int hd = 0; hd < 2; ++hd) {
        {
            f32x16 s0 = {};
#pragma unroll
            for (int j = 0; j < 8; ++j) { const bf16x8 kf = j < 3 ? R[j] : KFR(0, j); s0 = MFMA(kf, qf[j], s0); }
            float mx = max16(s0);
            if (pass) {
                const unsigned ln = lane_id();
                const char* kp = KK - wr * 2048 + hd * 262144 + (ln & 31) * 256 + (ln >> 5) * 16;
#pragma unroll 1
                for (int kb = 1; kb < 16; ++kb) {
                    s0 = f32x16{};
#pragma unroll
                    for (int d = 0; d < 8; ++d) s0 = MFMA(*(const bf16x8*)(kp + kb * 8192 + d * 32), qf[d], s0);
                    mx = __builtin_fmaxf(mx, max16(s0));
                }
            }
            const float m0_ = swapmax(mx);
#pragma unroll
            for (int i = 0; i < 16; ++i) ng[i] = -m0_;
            asm volatile("" : "+v"(ng) :: "memory"); SB();
        }
#pragma unroll
        for (int dt = 0; dt < 4; ++dt) o[dt] = f32x16{};
        l = 0.f;
#pragma unroll 1
    for (int tt = 8 * hd; tt < 8 * hd + 8; ++tt) {
        f32x16 s0, s1;
        u32x4 pw0, pw1, pw2, pw3;
        float ls = 0.f;
        {
            unsigned pv;
#pragma unroll
            for (int i = 0; i < 16; ++i) {
                const bf16x8 cur = R[i & 3];
                if (i == 3) pv = POLL();
                if (i == 5) { CHECK(pv); TM0(moth, 2, 0); SB(); }
                if (i == 14) { if (tt < 15) ARRIVE2(5, 2); else ARRIVE2(0, 2); }
                R[(i + 3) & 3] = i < 13 ? KFR(0, i + 3) : VFR(2, i - 13);
                if (i < 8) {
                    s0 = MFMA(cur, qf[i], i == 0 ? ng : s0);
                    if (i == 5) { PIN(s0); TLDV(0); } if (i == 6) TM0(moth, 2, 1); if (i == 7) { PIN(s0); TLDV(1); }
                    GAPEND(s0);
                } else {
                    s1 = MFMA(cur, qf[i - 8], i == 8 ? ng : s1);
                    if (i == 8) TM0(moth, 2, 2);
                    if (i == 9) { PIN(s0); EXPK(s0, 0, pw0, 0); EXPK(s0, 0, pw0, 1); asm volatile("" : "+v"(s0), "+v"(pw0)); TLDV(2); }
                    if ((i == 10 || i == 11)) { EXPK(s0, 0, pw0, i - 8); asm volatile("" : "+v"(s0), "+v"(pw0)); }
                    if (i >= 12) { EXPK(s0, 8, pw1, i - 12); ADD2(s0, 0, i - 12); if (i > 12) ADD2(s0, 8, i - 13);
                                   asm volatile("" : "+v"(s0), "+v"(pw1), "+v"(ls)); }
                    if (i == 13) { rdK0 += dlt; rdK1 += dlt; }
                    if (i == 14) TM0(moth, 2, 3); if (i == 15) TLDV(3);
                    GAPEND(s1);
                }
            }
            ADD2(s0, 8, 3);
        }
        {
            unsigned pv;
#pragma unroll
            for (int i = 0; i < 16; ++i) {
                const bf16x8 cur = R[i & 3];
                if (i == 3) pv = POLL();
                if (i == 5) { CHECK(pv); TM0(mcur, 0, 0); SB(); }
                if (i == 8) { ADD2(s1, 8, 3); l += ls; }
                if (i == 14) { if (tt < 14) ARRIVE2(5, 1); else ARRIVE2(2, 1); }
                R[(i + 3) & 3] = i < 13 ? VFR(2, i + 3) : KFR(0, i - 13);
                o[i & 3] = MFMA(cur, i < 4 ? PF(pw0) : i < 8 ? PF(pw1) : i < 12 ? PF(pw2) : PF(pw3), o[i & 3]);
                if (i == 1) { PIN(s1); EXPK(s1, 0, pw2, 0); EXPK(s1, 0, pw2, 1); asm volatile("" : "+v"(s1), "+v"(pw2)); }
                if ((i == 2 || i == 3)) { EXPK(s1, 0, pw2, i); asm volatile("" : "+v"(s1), "+v"(pw2)); }
                if (i >= 4 && i < 8) { EXPK(s1, 8, pw3, i - 4); ADD2(s1, 0, i - 4); if (i > 4) ADD2(s1, 8, i - 5);
                                       asm volatile("" : "+v"(s1), "+v"(pw3), "+v"(ls)); }
                if (i == 5) TLDK(0); if (i == 7) TM0(mcur, 0, 1);
                if (i == 8) { PIN(o[i & 3]); TLDK(1); } if (i == 10) TM0(mcur, 0, 2); if (i == 11) { PIN(o[i & 3]); TLDK(2); }
                if (i == 13) { rdV0 += dlt; rdV1 += dlt; }
                if (i == 14) TM0(mcur, 0, 3); if (i == 15) { PIN(o[i & 3]); TLDK(3); }
                GAPEND(o[i & 3]);
            }
        }
        pV += (tt == 6) ? 16384 + 131072 : 16384;
        pK += (tt == 5) ? 16384 + 131072 : 16384;
        { const unsigned t_ = mcur; mcur = moth; moth = t_; dlt = -dlt; }
    }
        {
            const float lt = swapsum(l);
            if (!(lt <= PBOUND)) *(volatile AS3 unsigned*)(size_t)flagw = 1u;
            const float inv = __builtin_amdgcn_rcpf(lt);
#pragma unroll
            for (int dt = 0; dt < 4; ++dt) {
#pragma unroll
                for (int i = 0; i < 16; ++i) o[dt][i] *= inv;
                qf[2 * dt] = sh[2 * dt]; qf[2 * dt + 1] = sh[2 * dt + 1];
                sh[2 * dt] = pack8<0>(o[dt]); sh[2 * dt + 1] = pack8<1>(o[dt]);
            }
            if (hd == 0) STAMP(3);
        }
    }
    STAMP(4);
    bf16x8 pt0[8], pt1[8];
    unsigned vP = lane_id() * 16; PIN(vP);
    vT = (vT & 0xff) | ((vT & 0x700) << 2); vT2 = vT + 32768;
    const char* wown = WO + g * 131072; const char* wpar = WO + (1 - g) * 131072;
#define WOT(c_, kt_) ((((kt_) >> 2) ? wpar : wown) + (((kt_) >> 1) & 1) * 262144 + ((kt_) & 1) * 65536 + (c_) * 256)
    {
        wait_cnt(cnt, arr);
        const unsigned e = wr * 2 * HALF + vP;
#pragma unroll
        for (int f = 0; f < 8; ++f) {
            *(AS3 bf16x8*)(size_t)(ring + e + f * 1024) = qf[f];
            *(AS3 bf16x8*)(size_t)(ring + e + HALF + f * 1024) = sh[f];
        }
        LBAR();
        const unsigned oring = lds0 + (1 - g) * RINGB;
#pragma unroll
        for (int f = 0; f < 8; ++f) { pt0[f] = lds128(oring + e + f * 1024); pt1[f] = lds128(oring + e + HALF + f * 1024); }
        LBAR();
#pragma unroll
        for (int t = 0; t < 3; ++t) {
            const char* w = WOT(0, t);
#pragma unroll
            for (int j = 0; j < 4; ++j) { TM0(mbase, 2 * t, j); NOP0(); TLD_WO(w, j); }
        }
        ARRIVE(6);
        wait_cnt(cnt, arr);
#pragma unroll
        for (int j = 0; j < 3; ++j) R[j] = VFR(0, j);
    }
    STAMP(5);

    char* pw = part + ((size_t)(idx * 8 + hg) * 4 + wr) * 32768 + g * 16384;
#pragma unroll 1
    for (int c = 0; c < 2; ++c) {
        f32x16 oa[4] = {};
#define OSTEP(kt, OF) do { \
            constexpr int B = (2 * (kt)) & 7; \
            asm volatile("" : "+s"(wown), "+s"(wpar));                     \
            const char* wn = (kt) < 5 ? WOT(c, (kt) + 3) : WOT(1, (kt) - 5);     \
            unsigned pv; \
            _Pragma("unroll") for (int i = 0; i < 16; ++i) { \
                const bf16x8 cur = R[i & 3]; \
                if (i == 3) pv = POLL(); \
                if (i == 5) { CHECK(pv); TM0(mbase, (B + 6) & 7, 0); SB(); } \
                if (i == 14) { if ((kt) == 0) { if (c == 0) ARRIVE2(5, 2); else ARRIVE2(13, 2); } else ARRIVE2(5, 2); } \
                R[(i + 3) & 3] = i < 13 ? VFR(B, i + 3) : VFR((B + 2) & 7, i - 13); \
                oa[i & 3] = MFMA(cur, OF[((kt) & 1) * 4 + (i >> 2)], oa[i & 3]); \
                if (i == 5 || i == 8 || i == 11 || i == 15) { PIN(oa[i & 3]); TLD_WO(wn, i == 5 ? 0 : i == 8 ? 1 : i == 11 ? 2 : 3); } \
                if (i == 7) TM0(mbase, (B + 6) & 7, 1); if (i == 10) TM0(mbase, (B + 6) & 7, 2); if (i == 14) TM0(mbase, (B + 6) & 7, 3); \
                GAPEND(oa[i & 3]); \
            } } while (0)
        OSTEP(0, qf); OSTEP(1, qf); OSTEP(2, sh); OSTEP(3, sh); OSTEP(4, pt0); OSTEP(5, pt0); OSTEP(6, pt1); OSTEP(7, pt1);
#pragma unroll
        for (int nt = 0; nt < 4; ++nt) {
            *(bf16x8*)(pw + (nt * 2 + 0) * 1024 + vP) = pack8<0>(oa[nt]);
            *(bf16x8*)(pw + (nt * 2 + 1) * 1024 + vP) = pack8<1>(oa[nt]);
        }
        asm volatile("" ::: "memory"); SB();
        pw += 8192;
        if (c == 0) STAMP(6);
    }
    asm volatile("s_waitcnt vmcnt(0)" ::: "memory");
    LBAR();
    if (pass == 1 || __builtin_amdgcn_readfirstlane(*(volatile AS3 unsigned*)(size_t)flagw) == 0) break;
    LBAR();
    if (wave == 0) *(AS3 unsigned*)(size_t)(lds0 + 2 * RINGB + lane_id() * 4) = 0;
    }
}

__global__ __launch_bounds__(256) void reduce_out(const char* __restrict__ part, unsigned short* __restrict__ out) {
    const int gid = blockIdx.x * 256 + threadIdx.x;
    const int lane = gid & 63, fr = (gid >> 6) & 31, wr = (gid >> 11) & 3, idx = gid >> 13;
    float acc[8] = {};
#pragma unroll
    for (int hg = 0; hg < 8; ++hg) {
        const u32x4 v = *(const u32x4*)(part + ((size_t)(idx * 8 + hg) * 4 + wr) * 32768 + fr * 1024 + lane * 16);
#pragma unroll
        for (int j = 0; j < 4; ++j) {
            acc[2 * j] += __uint_as_float(v[j] << 16);
            acc[2 * j + 1] += __uint_as_float(v[j] & 0xffff0000u);
        }
    }
    const int blk = idx >> 4, b = idx & 15, h = lane >> 5;
    const int row = wr * 32 + (lane & 31);
    const int n0 = fr * 16 + 4 * h;
    unsigned short* o = out + ((size_t)(b * 128 + row)) * 1024 + blk * 512 + n0;
    u32x2 w0 = {cvtpk(acc[0], acc[1]), cvtpk(acc[2], acc[3])};
    u32x2 w1 = {cvtpk(acc[4], acc[5]), cvtpk(acc[6], acc[7])};
    *(u32x2*)(o) = w0;
    *(u32x2*)(o + 8) = w1;
}

extern "C" void kernel_launch(void* const* d_in, const int* in_sizes, int n_in, void* d_out, int out_size, void* d_ws,
                              size_t ws_size, hipStream_t stream) {
    char* part = (char*)d_ws;
    fused_attn<<<256, 512, 0, stream>>>((const char*)d_in[0], (const char*)d_in[1], (const char*)d_in[2],
                                        (const char*)d_in[3], (const char*)d_in[4], (const char*)d_in[5],
                                        (const char*)d_in[6], (const char*)d_in[7], (const char*)d_in[8],
                                        (const char*)d_in[9], part);
    reduce_out<<<1024, 256, 0, stream>>>(part, (unsigned short*)d_out);
}
```

```cpp
#include <hip/hip_runtime.h>
#include <hip/hip_bf16.h>

typedef short bf16x8 __attribute__((ext_vector_type(8)));
typedef short s16x4 __attribute__((ext_vector_type(4)));
typedef float f32x16 __attribute__((ext_vector_type(16)));
typedef float f32x2 __attribute__((ext_vector_type(2)));
typedef __bf16 bf16x2_t __attribute__((ext_vector_type(2)));
typedef unsigned u32x4 __attribute__((ext_vector_type(4)));
typedef unsigned u32x2 __attribute__((ext_vector_type(2)));
#define AS3 __attribute__((address_space(3)))
#define DI __device__ __forceinline__

#define ALT 0
#define SETP(par) do { if (g == (par)) __builtin_amdgcn_s_setprio(1); else __builtin_amdgcn_s_setprio(0); } while (0)
constexpr int HALF = 8192;
constexpr int RINGB = 9 * HALF;
constexpr float C2 = 0.088388347648318440f * 1.4426950408889634f;
#define PBOUND 1048576.0f

#define MFMA(a, b, c) __builtin_amdgcn_mfma_f32_32x32x16_bf16((a), (b), (c), 0, 0, 0)
#define LBAR() asm volatile("s_waitcnt lgkmcnt(0)\n\ts_barrier" ::: "memory")
#define SB() __builtin_amdgcn_sched_barrier(0)
#define PIN(x) asm volatile("" : "+v"(x))
#define GAPEND(acc) do { asm volatile("" : "+v"(acc) :: "memory"); SB(); } while (0)
#define MX3(a, b, c) __builtin_fmaxf(__builtin_fmaxf((a), (b)), (c))
#define EXP2(x) __builtin_amdgcn_exp2f(x)

DI unsigned cvtpk(float lo, float hi) {
    f32x2 v = {lo, hi};
    bf16x2_t b = __builtin_convertvector(v, bf16x2_t);
    return __builtin_bit_cast(unsigned, b);
}
template <int S> DI bf16x8 pack8(const f32x16& x) {
    u32x4 p = {cvtpk(x[8 * S + 0], x[8 * S + 1]), cvtpk(x[8 * S + 2], x[8 * S + 3]),
               cvtpk(x[8 * S + 4], x[8 * S + 5]), cvtpk(x[8 * S + 6], x[8 * S + 7])};
    return __builtin_bit_cast(bf16x8, p);
}
DI bf16x8 lds128(unsigned a) { return *(const AS3 bf16x8*)(size_t)(a); }
DI s16x4 ldstr(unsigned a) {
    return __builtin_bit_cast(s16x4, __builtin_amdgcn_ds_read_tr16_b64_v4i16((AS3 s16x4*)(size_t)(a)));
}
DI bf16x8 cat(s16x4 lo, s16x4 hi) { return __builtin_shufflevector(lo, hi, 0, 1, 2, 3, 4, 5, 6, 7); }
DI bf16x8 ldtr2(unsigned a0, unsigned a1) { return cat(ldstr(a0), ldstr(a1)); }
DI float swapmax(float x) {
    auto r = __builtin_amdgcn_permlane32_swap(__float_as_uint(x), __float_as_uint(x), false, false);
    return __builtin_fmaxf(__uint_as_float(r[0]), __uint_as_float(r[1]));
}
DI float swapsum(float x) {
    auto r = __builtin_amdgcn_permlane32_swap(__float_as_uint(x), __float_as_uint(x), false, false);
    return __uint_as_float(r[0]) + __uint_as_float(r[1]);
}
DI float max16(const f32x16& s) {
    float a = MX3(s[0], s[1], s[2]), b = MX3(s[3], s[4], s[5]);
    a = MX3(a, s[6], s[7]); b = MX3(b, s[8], s[9]);
    a = MX3(a, s[10], s[11]); b = MX3(b, s[12], s[13]);
    a = MX3(a, s[14], s[15]);
    return __builtin_fmaxf(a, b);
}
DI unsigned lane_id() { return __builtin_amdgcn_mbcnt_hi(~0u, __builtin_amdgcn_mbcnt_lo(~0u, 0u)); }

#define M0SET(base, off) asm volatile("s_add_u32 m0, %0, %1" :: "s"(base), "i"(off) : "scc")
#define GLD(voff, sb, imm) asm volatile("global_load_lds_dwordx4 %0, %1 offset:%2" :: "v"(voff), "s"(sb), "i"(imm) : "memory")
#define GLDNT(voff, sb, imm) asm volatile("global_load_lds_dwordx4 %0, %1 offset:%2 nt" :: "v"(voff), "s"(sb), "i"(imm) : "memory")
#define NOP0() asm volatile("s_nop 0")

#define ARRIVE(N) ARRIVE2(N, 0)
#define ARRIVE2(N, LG) do { unsigned long long sv_; asm volatile( \
        "s_waitcnt vmcnt(" #N ") lgkmcnt(" #LG ")\n\t" \
        "s_mov_b64 %0, exec\n\t" \
        "s_mov_b64 exec, 1\n\t" \
        "ds_add_u32 %1, %1\n\t" \
        "s_mov_b64 exec, %0" : "=&s"(sv_) : "v"(cnt) : "memory"); arr += arrinc; } while (0)
DI void wait_cnt(unsigned cnt, unsigned target) {
    unsigned v, s;
    asm volatile(
        "1:\n\t"
        "ds_read_b32 %0, %2\n\t"
        "s_waitcnt lgkmcnt(0)\n\t"
        "v_readfirstlane_b32 %1, %0\n\t"
        "s_nop 0\n\t"
        "s_cmp_ge_u32 %1, %3\n\t"
        "s_cbranch_scc0 1b"
        : "=&v"(v), "=&s"(s) : "v"(cnt), "s"(target) : "memory", "scc");
}
#define POLL() (*(volatile AS3 unsigned*)(size_t)cnt)
#define CHECK(pv) do { if (__builtin_expect((unsigned)__builtin_amdgcn_readfirstlane(pv) < arr, 0)) wait_cnt(cnt, arr); \
                       asm volatile("" ::: "memory"); SB(); } while (0)

#define STAMP(i)

__global__ __launch_bounds__(512, 2) void fused_attn(const char* __restrict__ xA, const char* __restrict__ kA,
                                                     const char* __restrict__ vA, const char* __restrict__ wqA,
                                                     const char* __restrict__ woA, const char* __restrict__ xB,
                                                     const char* __restrict__ kB, const char* __restrict__ vB,
                                                     const char* __restrict__ wqB, const char* __restrict__ woB,
                                                     char* __restrict__ part) {
    __shared__ __attribute__((aligned(1024))) char lds[2 * RINGB + 1024 + 2048];
    const int tid = threadIdx.x;
    const int wave = __builtin_amdgcn_readfirstlane(tid >> 6);
    const int g = wave >> 2, wr = wave & 3;
    const int bid = blockIdx.x;

    const unsigned lds0 = (unsigned)(size_t)(AS3 char*)lds;
    const unsigned ring = lds0 + g * RINGB;
    const unsigned mbase = ring + wr * 2048;
    unsigned cnt = lds0 + 2 * RINGB + g * 128;
    const unsigned arrinc = 4 * cnt;
    PIN(cnt);
    if (tid < 64) *(AS3 unsigned*)(size_t)(lds0 + 2 * RINGB + tid * 4) = 0;

    STAMP(0);
    const unsigned flagw = lds0 + 2 * RINGB + 64;
#pragma unroll 1
    for (int pass = 0; pass < 2; ++pass) {
    unsigned arr = 0;
    int bid_ = bid; asm volatile("" : "+s"(bid_));
    const int hg = bid_ & 7, idx = bid_ >> 3, blk = idx >> 4, b = idx & 15;
    const char* X = (blk ? xB : xA) + (size_t)b * 131072 + wr * 32768;
    const char* WQ = (blk ? wqB : wqA) + (size_t)wr * 65536 + (4 * hg + g) * 256;
    const char* KK = (blk ? kB : kA) + ((size_t)b * 32 + 4 * hg + g) * 131072 + wr * 2048;
    const char* VV = (blk ? vB : vA) + ((size_t)b * 32 + 4 * hg + g) * 131072 + wr * 2048;
    const char* WO = (blk ? woB : woA) + (size_t)(4 * hg) * 131072 + wr * 8192 + g * 512;

    unsigned lane = lane_id(); PIN(lane);
    const unsigned r32 = lane & 31, h = lane >> 5;
    const unsigned colb = (unsigned)(h * 64 + 16 * ((lane & 3) ^ ((2 * wr + ((lane >> 4) & 1)) & 3)));
    const unsigned rowl = (lane >> 2) & 7;
    const unsigned vWQ = rowl * 8192 + colb;
    const unsigned vX = (lane >> 2) * 1024 + 16 * ((lane & 3) ^ ((lane >> 4) & 3)), vX2 = vX + 16384;
    const unsigned blkb = (lane >> 4) & 1, q4 = (lane & 15) >> 2, p4 = lane & 3;
    const unsigned xr = (r32 >> 2) & 3;
    unsigned rdK0 = ring + 2048 * (r32 >> 3) + 64 * (r32 & 7) + 16 * (h ^ xr), rdK1 = rdK0 ^ 32;
    unsigned rdV0 = ring + 64 * (4 * h + q4) + 16 * ((2 * blkb + (p4 >> 1)) ^ h) + 8 * (p4 & 1), rdV1 = rdV0 ^ 32;
    const unsigned rdW0 = ring + 2048 * h + 64 * q4 + 16 * ((2 * blkb + (p4 & 1)) ^ (2 * h)) + 8 * (p4 >> 1), rdW1 = rdW0 ^ 16;
    const unsigned rdW0x = rdW0 + 8 * HALF, rdW1x = rdW1 + 8 * HALF;
    const unsigned rdX0 = ring + wr * 2048 + 64 * r32 + 16 * (h ^ xr), rdX1 = rdX0 ^ 32;

    unsigned vT = rowl * 256 + colb, vT2 = vT + 8192;

    bf16x8 R[4], xf0, xf1;
#define WRD(s, ks, j) ((s) == 8 ? ldtr2(rdW0x + 4096 * (ks) + 512 * (j), rdW1x + 4096 * (ks) + 512 * (j) + 256) \
                                : ldtr2(rdW0 + (s) * HALF + 4096 * (ks) + 512 * (j), rdW1 + (s) * HALF + 4096 * (ks) + 512 * (j) + 256))
#define KRD(s, d) (((d) & 1) ? lds128(rdK1 + (s) * HALF + 512 * ((d) >> 1)) : lds128(rdK0 + (s) * HALF + 512 * ((d) >> 1)))
#define VRD(s, k1, j) ldtr2(rdV0 + (s) * HALF + 4096 * (k1) + 512 * (j), rdV1 + (s) * HALF + 4096 * (k1) + 2048 + 512 * (j))

#define QFR(s, i) WRD((s) + 1 + ((i) >> 3), ((i) >> 2) & 1, (i) & 3)
#define KFR(s, i) KRD((s) + ((i) >> 3), (i) & 7)
#define VFR(s, i) VRD((s) + ((i) >> 3), ((i) >> 2) & 1, (i) & 3)

#pragma unroll
    for (int u = 0; u < 3; ++u) {
        const int s = 3 * ((u + 2) % 3);
        const char* px = X + u * 64; const char* pw = WQ + (size_t)u * 262144;
        M0SET(mbase, s * HALF); NOP0(); GLD(vX, px, 0); M0SET(mbase, s * HALF + 1024); NOP0(); GLD(vX2, px, 0);
        M0SET(mbase, (s + 1) * HALF); NOP0(); GLD(vWQ, pw, 0); M0SET(mbase, (s + 1) * HALF + 896); NOP0(); GLD(vWQ, pw, 128);
        M0SET(mbase, (s + 2) * HALF - 512); NOP0(); GLD(vWQ, pw, 512); M0SET(mbase, (s + 2) * HALF + 896 - 512); NOP0(); GLD(vWQ, pw, 640);
    }
    LBAR();
    ARRIVE(12);
    wait_cnt(cnt, arr);
    STAMP(1);
    {
        const unsigned mw = lds0 + 2 * RINGB + 1024 + wave * 256, vl = lane * 64;
        const char* w2 = (blk ? woB : woA) + (size_t)(4 * hg) * 131072 + b * 32768 + wave * 4096;
        asm volatile("s_mov_b32 m0, %0" :: "s"(mw)); NOP0();
        asm volatile("global_load_lds_dword %0, %1" :: "v"(vl), "s"(w2) : "memory");
    }
    xf0 = lds128(rdX0 + 6 * HALF);
#pragma unroll
    for (int j = 0; j < 3; ++j) R[j] = QFR(6, j);

    bf16x8 qf[8], sh[8];
    {
        f32x16 qa[4] = {}, qb[4] = {};
        const char* px = X + 3 * 64; const char* pw = WQ + (size_t)3 * 262144;
#define QM0(MODE, sp, p) do { if (MODE == 1) { \
                    M0SET(mbase, (p) == 0 ? (sp) * HALF : (p) == 1 ? (sp) * HALF + 1024 : (p) == 2 ? ((sp) + 1) * HALF : (p) == 3 ? ((sp) + 1) * HALF + 896 \
                          : (p) == 4 ? ((sp) + 2) * HALF - 512 : ((sp) + 2) * HALF + 896 - 512); \
                } else if (MODE >= 2) { M0SET(mbase, ((sp) + ((p) >> 1)) * HALF + ((p) & 1) * 896); } } while (0)
#define QLD(MODE, p) do { if (MODE == 1) { \
                    if ((p) == 0) GLD(vX, px, 0); else if ((p) == 1) GLD(vX2, px, 0); else if ((p) == 2) GLD(vWQ, pw, 0); \
                    else if ((p) == 3) GLD(vWQ, pw, 128); else if ((p) == 4) GLD(vWQ, pw, 512); else GLD(vWQ, pw, 640); \
                } else if (MODE == 2) { \
                    if ((p) < 2) GLDNT(vT, KK, ((p) & 1) * 128); else if ((p) < 4) GLDNT(vT2, KK, ((p) & 1) * 128); else GLDNT(vT, VV, ((p) & 1) * 128); \
                } else if (MODE == 3) { \
                    const char* k1_ = KK + 16384; \
                    if ((p) < 2) GLDNT(vT2, VV, ((p) & 1) * 128); else if ((p) < 4) GLDNT(vT, k1_, ((p) & 1) * 128); else GLDNT(vT2, k1_, ((p) & 1) * 128); \
                } } while (0)
#define QP(acc, MODE, p) do { if (MODE) { PIN(acc); QLD(MODE, p); } } while (0)
#define QSTEP(U3, MODE) do { \
            constexpr int s = 3 * (((U3) + 2) % 3), sp = 3 * (((U3) + 1) % 3), sn = 3 * ((U3) % 3); \
            unsigned pv; \
            QM0(MODE, sp, 0); SB(); \
            xf1 = lds128(rdX1 + s * HALF); \
            _Pragma("unroll") for (int i = 0; i < 16; ++i) { \
                const bf16x8 cur = R[i & 3]; \
                if ((ALT & 4) && (i & 7) == 0) SETP((i >> 3) & 1); \
                if (i == 13) { ARRIVE(6); pv = POLL(); } \
                if (i == 15) { CHECK(pv); \
                    if (MODE != 3) { xf0 = lds128(rdX0 + sn * HALF); _Pragma("unroll") for (int j = 0; j < 3; ++j) R[j] = QFR(sn, j); } \
                    else { _Pragma("unroll") for (int j = 0; j < 3; ++j) R[j] = KFR(0, j); } } \
                if (i < 13) R[(i + 3) & 3] = QFR(s, i + 3); \
                if (i < 8) { qa[i & 3] = MFMA(cur, (i & 4) ? xf1 : xf0, qa[i & 3]); \
                    if (!(i & 1)) QP(qa[i & 3], MODE, i >> 1); else QM0(MODE, sp, (i >> 1) + 1); \
                    GAPEND(qa[i & 3]); } \
                else { qb[i & 3] = MFMA(cur, (i & 4) ? xf1 : xfk, qb[i & 3]); \
                    if (i < 12) { if (!(i & 1)) QP(qb[i & 3], MODE, i >> 1); else if (i == 9) QM0(MODE, sp, 5); } \
                    GAPEND(qb[i & 3]); } \
            } \
            if (MODE == 1) { px += 64; pw += 262144; } \
        } while (0)
#define xfk xf0
        QSTEP(0, 0);
#pragma unroll 1
        for (int i = 0; i < 4; ++i) { QSTEP(1, 1); QSTEP(2, 1); QSTEP(0, 1); }
        QSTEP(1, 1); QSTEP(2, 2); QSTEP(0, 3);
#pragma unroll
        for (int dt = 0; dt < 4; ++dt) {
#pragma unroll
            for (int i = 0; i < 16; ++i) { qa[dt][i] *= C2; qb[dt][i] *= C2; }
            qf[2 * dt] = pack8<0>(qa[dt]); qf[2 * dt + 1] = pack8<1>(qa[dt]);
            sh[2 * dt] = pack8<0>(qb[dt]); sh[2 * dt + 1] = pack8<1>(qb[dt]);
        }
    }
    STAMP(2);

    f32x16 o[4];
    f32x16 ng;
    float l = 0.f;
    const char* pV = VV + 16384;
    const char* pK = KK + 32768;
    unsigned mcur = mbase, moth = mbase + 4 * HALF;
    int dlt = 4 * HALF;
#define PF(w) __builtin_bit_cast(bf16x8, w)
#define TM0(MB, SL, j) M0SET(MB, ((SL) + ((j) >> 1)) * HALF + ((j) & 1) * 896)
#define TLD(P, j) do { if ((j) < 2) GLDNT(vT, P, ((j) & 1) * 128); else GLDNT(vT2, P, ((j) & 1) * 128); } while (0)
#define TLDV(j) TLD(pV, j)
#define TLDK(j) TLD(pK, j)
#define TLD_WO(P, j) do { if ((j) < 2) GLD(vT, P, ((j) & 1) * 128); else GLD(vT2, P, ((j) & 1) * 128); } while (0)
#define EXPK(S, B, PW, j) do { S[(B) + 2 * (j)] = EXP2(S[(B) + 2 * (j)]); S[(B) + 2 * (j) + 1] = EXP2(S[(B) + 2 * (j) + 1]); \
                               PW[j] = cvtpk(S[(B) + 2 * (j)], S[(B) + 2 * (j) + 1]); } while (0)
#define ADD2(S, B, j) do { ls += S[(B) + 2 * (j)]; ls += S[(B) + 2 * (j) + 1]; } while (0)
#pragma unroll 1
    for (int hd = 0; hd < 2; ++hd) {
        {
            f32x16 s0 = {};
#pragma unroll
            for (int j = 0; j < 8; ++j) { const bf16x8 kf = j < 3 ? R[j] : KFR(0, j); s0 = MFMA(kf, qf[j], s0); }
            float mx = max16(s0);
            if (pass) {
                const unsigned ln = lane_id();
                const char* kp = KK - wr * 2048 + hd * 262144 + (ln & 31) * 256 + (ln >> 5) * 16;
#pragma unroll 1
                for (int kb = 1; kb < 16; ++kb) {
                    s0 = f32x16{};
#pragma unroll
                    for (int d = 0; d < 8; ++d) s0 = MFMA(*(const bf16x8*)(kp + kb * 8192 + d * 32), qf[d], s0);
                    mx = __builtin_fmaxf(mx, max16(s0));
                }
            }
            const float m0_ = swapmax(mx);
#pragma unroll
            for (int i = 0; i < 16; ++i) ng[i] = -m0_;
            asm volatile("" : "+v"(ng) :: "memory"); SB();
        }
#pragma unroll
        for (int dt = 0; dt < 4; ++dt) o[dt] = f32x16{};
        l = 0.f;
#pragma unroll 1
    for (int tt = 8 * hd; tt < 8 * hd + 8; ++tt) {
        f32x16 s0, s1;
        u32x4 pw0, pw1, pw2, pw3;
        float ls = 0.f;
        if (ALT & 8) SETP(0);
        if ((ALT & 12) == 4) __builtin_amdgcn_s_setprio(0);
        {
            unsigned pv;
#pragma unroll
            for (int i = 0; i < 16; ++i) {
                const bf16x8 cur = R[i & 3];
                if (i == 3) pv = POLL();
                if (i == 5) { CHECK(pv); TM0(moth, 2, 0); SB(); }
                if (i == 14) ARRIVE2(5, 2);
                R[(i + 3) & 3] = i < 13 ? KFR(0, i + 3) : VFR(2, i - 13);
                if (i < 8) {
                    s0 = MFMA(cur, qf[i], i == 0 ? ng : s0);
                    if (i == 5) { PIN(s0); TLDV(0); } if (i == 6) TM0(moth, 2, 1); if (i == 7) { PIN(s0); TLDV(1); }
                    GAPEND(s0);
                } else {
                    s1 = MFMA(cur, qf[i - 8], i == 8 ? ng : s1);
                    if (i == 8) TM0(moth, 2, 2);
                    if (i == 9) { PIN(s0); EXPK(s0, 0, pw0, 0); EXPK(s0, 0, pw0, 1); asm volatile("" : "+v"(s0), "+v"(pw0)); TLDV(2); }
                    if ((i == 10 || i == 11)) { EXPK(s0, 0, pw0, i - 8); asm volatile("" : "+v"(s0), "+v"(pw0)); }
                    if (i >= 12) { EXPK(s0, 8, pw1, i - 12); ADD2(s0, 0, i - 12); if (i > 12) ADD2(s0, 8, i - 13);
                                   asm volatile("" : "+v"(s0), "+v"(pw1), "+v"(ls)); }
                    if (i == 13) { rdK0 += dlt; rdK1 += dlt; }
                    if (i == 14) TM0(moth, 2, 3); if (i == 15) TLDV(3);
                    GAPEND(s1);
                }
            }
            ADD2(s0, 8, 3);
        }
        if (tt == 14) { vT = (vT & 0xff) | ((vT & 0x700) << 2); vT2 = vT + 32768; pK = WO + g * 131072; }
        if (ALT & 8) SETP(1);
        {
            unsigned pv;
#pragma unroll
            for (int i = 0; i < 16; ++i) {
                const bf16x8 cur = R[i & 3];
                if (i == 3) pv = POLL();
                if (i == 5) { CHECK(pv); TM0(mcur, 0, 0); SB(); }
                if (i == 8) { ADD2(s1, 8, 3); l += ls; }
                if (i == 14) ARRIVE2(5, 1);
                R[(i + 3) & 3] = i < 13 ? VFR(2, i + 3) : KFR(0, i - 13);
                o[i & 3] = MFMA(cur, i < 4 ? PF(pw0) : i < 8 ? PF(pw1) : i < 12 ? PF(pw2) : PF(pw3), o[i & 3]);
                if (i == 1) { PIN(s1); EXPK(s1, 0, pw2, 0); EXPK(s1, 0, pw2, 1); asm volatile("" : "+v"(s1), "+v"(pw2)); }
                if ((i == 2 || i == 3)) { EXPK(s1, 0, pw2, i); asm volatile("" : "+v"(s1), "+v"(pw2)); }
                if (i >= 4 && i < 8) { EXPK(s1, 8, pw3, i - 4); ADD2(s1, 0, i - 4); if (i > 4) ADD2(s1, 8, i - 5);
                                       asm volatile("" : "+v"(s1), "+v"(pw3), "+v"(ls)); }
                if (i == 5) TLDK(0); if (i == 7) TM0(mcur, 0, 1);
                if (i == 8) { PIN(o[i & 3]); TLDK(1); } if (i == 10) TM0(mcur, 0, 2); if (i == 11) { PIN(o[i & 3]); TLDK(2); }
                if (i == 13) { rdV0 += dlt; rdV1 += dlt; }
                if (i == 14) TM0(mcur, 0, 3); if (i == 15) { PIN(o[i & 3]); TLDK(3); }
                GAPEND(o[i & 3]);
            }
        }
        pV = (tt == 14) ? pK + 65536 : pV + ((tt == 6) ? 16384 + 131072 : 16384);
        pK += (tt == 14) ? 262144 : (tt == 5) ? 16384 + 131072 : 16384;
        { const unsigned t_ = mcur; mcur = moth; moth = t_; dlt = -dlt; }
    }
        {
            const float lt = swapsum(l);
            if (!(lt <= PBOUND)) *(volatile AS3 unsigned*)(size_t)flagw = 1u;
            const float inv = __builtin_amdgcn_rcpf(lt);
#pragma unroll
            for (int dt = 0; dt < 4; ++dt) {
#pragma unroll
                for (int i = 0; i < 16; ++i) o[dt][i] *= inv;
                qf[2 * dt] = sh[2 * dt]; qf[2 * dt + 1] = sh[2 * dt + 1];
                sh[2 * dt] = pack8<0>(o[dt]); sh[2 * dt + 1] = pack8<1>(o[dt]);
            }
            if (hd == 0) STAMP(3);
        }
    }
    STAMP(4);
    bf16x8 pt0[8], pt1[8];
    unsigned vP = lane_id() * 16; PIN(vP);
    const char* wown = WO + g * 131072; const char* wpar = WO + (1 - g) * 131072;
#define WOT(c_, kt_) ((((kt_) >> 2) ? wpar : wown) + (((kt_) >> 1) & 1) * 262144 + ((kt_) & 1) * 65536 + (c_) * 256)
    {
        ARRIVE(0);
        wait_cnt(cnt, arr);
        const unsigned e = wr * 2 * HALF + vP;
#pragma unroll
        for (int f = 0; f < 8; ++f) {
            *(AS3 bf16x8*)(size_t)(ring + e + f * 1024) = qf[f];
            *(AS3 bf16x8*)(size_t)(ring + e + HALF + f * 1024) = sh[f];
        }
        LBAR();
        const unsigned oring = lds0 + (1 - g) * RINGB;
#pragma unroll
        for (int f = 0; f < 8; ++f) { pt0[f] = lds128(oring + e + f * 1024); pt1[f] = lds128(oring + e + HALF + f * 1024); }
        LBAR();
#pragma unroll
        for (int t = 0; t < 3; ++t) {
            const char* w = WOT(0, t);
#pragma unroll
            for (int j = 0; j < 4; ++j) { TM0(mbase, 2 * t, j); NOP0(); TLD_WO(w, j); }
        }
        ARRIVE(6);
        wait_cnt(cnt, arr);
#pragma unroll
        for (int j = 0; j < 3; ++j) R[j] = VFR(0, j);
    }
    STAMP(5);

    char* pw = part + ((size_t)(idx * 8 + hg) * 4 + wr) * 32768 + g * 16384;
#pragma unroll 1
    for (int c = 0; c < 2; ++c) {
        f32x16 oa[4] = {};
#define OSTEP(kt, OF) do { \
            constexpr int B = (2 * (kt)) & 7; \
            asm volatile("" : "+s"(wown), "+s"(wpar));                     \
            const char* wn = (kt) < 5 ? WOT(c, (kt) + 3) : WOT(1, (kt) - 5);     \
            unsigned pv; \
            _Pragma("unroll") for (int i = 0; i < 16; ++i) { \
                const bf16x8 cur = R[i & 3]; \
                if ((ALT & 1) && i == 0) SETP((kt) & 1); \
                if ((ALT & 2) && (i & 7) == 0) SETP((i >> 3) & 1); \
                if (i == 3) pv = POLL(); \
                if (i == 5) { CHECK(pv); TM0(mbase, (B + 6) & 7, 0); SB(); } \
                if (i == 14) { if ((kt) == 0) { if (c == 0) ARRIVE2(5, 2); else ARRIVE2(13, 2); } else ARRIVE2(5, 2); } \
                R[(i + 3) & 3] = i < 13 ? VFR(B, i + 3) : VFR((B + 2) & 7, i - 13); \
                oa[i & 3] = MFMA(cur, OF[((kt) & 1) * 4 + (i >> 2)], oa[i & 3]); \
                if (i == 5 || i == 8 || i == 11 || i == 15) { PIN(oa[i & 3]); TLD_WO(wn, i == 5 ? 0 : i == 8 ? 1 : i == 11 ? 2 : 3); } \
                if (i == 7) TM0(mbase, (B + 6) & 7, 1); if (i == 10) TM0(mbase, (B + 6) & 7, 2); if (i == 14) TM0(mbase, (B + 6) & 7, 3); \
                GAPEND(oa[i & 3]); \
            } } while (0)
        OSTEP(0, qf); OSTEP(1, qf); OSTEP(2, sh); OSTEP(3, sh); OSTEP(4, pt0); OSTEP(5, pt0); OSTEP(6, pt1); OSTEP(7, pt1);
#pragma unroll
        for (int nt = 0; nt < 4; ++nt) {
            *(bf16x8*)(pw + (nt * 2 + 0) * 1024 + vP) = pack8<0>(oa[nt]);
            *(bf16x8*)(pw + (nt * 2 + 1) * 1024 + vP) = pack8<1>(oa[nt]);
        }
        asm volatile("" ::: "memory"); SB();
        pw += 8192;
        if (c == 0) STAMP(6);
    }
    asm volatile("s_waitcnt vmcnt(0)" ::: "memory");
    LBAR();
    if (pass == 1 || __builtin_amdgcn_readfirstlane(*(volatile AS3 unsigned*)(size_t)flagw) == 0) break;
    LBAR();
    if (wave == 0) *(AS3 unsigned*)(size_t)(lds0 + 2 * RINGB + lane_id() * 4) = 0;
    }
}

__global__ __launch_bounds__(256) void reduce_out(const char* __restrict__ part, unsigned short* __restrict__ out) {
    __shared__ __attribute__((aligned(16))) char tile[32 * 272];
    const int tid = threadIdx.x, lane = tid & 63, w = tid >> 6;
    const int c = blockIdx.x & 3, wr = (blockIdx.x >> 2) & 3, idx = blockIdx.x >> 4;
    const char* src = part + ((size_t)(idx * 8) * 4 + wr) * 32768 + (c * 8 + w * 2) * 1024 + lane * 16;
    u32x4 v[2][8];
#pragma unroll
    for (int hg = 0; hg < 8; ++hg) {
        v[0][hg] = *(const u32x4*)(src + (size_t)hg * 131072);
        v[1][hg] = *(const u32x4*)(src + (size_t)hg * 131072 + 1024);
    }
#pragma unroll
    for (int s = 0; s < 2; ++s) {
        float acc[8] = {};
#pragma unroll
        for (int hg = 0; hg < 8; ++hg)
#pragma unroll
            for (int j = 0; j < 4; ++j) {
                acc[2 * j] += __uint_as_float(v[s][hg][j] << 16);
                acc[2 * j + 1] += __uint_as_float(v[s][hg][j] & 0xffff0000u);
            }
        char* t = tile + (lane & 31) * 272 + (32 * w + 16 * s + 4 * (lane >> 5)) * 2;
        *(u32x2*)(t) = u32x2{cvtpk(acc[0], acc[1]), cvtpk(acc[2], acc[3])};
        *(u32x2*)(t + 16) = u32x2{cvtpk(acc[4], acc[5]), cvtpk(acc[6], acc[7])};
    }
    __syncthreads();
    const int blk = idx >> 4, b = idx & 15;
#pragma unroll
    for (int p = 0; p < 2; ++p) {
        const int row = p * 16 + (tid >> 4), ch = tid & 15;
        const u32x4 o = *(const u32x4*)(tile + row * 272 + ch * 16);
        *(u32x4*)((char*)out + ((size_t)(b * 128 + wr * 32 + row) * 1024 + blk * 512 + c * 128) * 2 + ch * 16) = o;
    }
}

extern "C" void kernel_launch(void* const* d_in, const int* in_sizes, int n_in, void* d_out, int out_size, void* d_ws,
                              size_t ws_size, hipStream_t stream) {
    char* part = (char*)d_ws;
    fused_attn<<<256, 512, 0, stream>>>((const char*)d_in[0], (const char*)d_in[1], (const char*)d_in[2],
                                        (const char*)d_in[3], (const char*)d_in[4], (const char*)d_in[5],
                                        (const char*)d_in[6], (const char*)d_in[7], (const char*)d_in[8],
                                        (const char*)d_in[9], part);
    reduce_out<<<512, 256, 0, stream>>>(part, (unsigned short*)d_out);
}
```

```cpp
#include <hip/hip_runtime.h>
#include <hip/hip_bf16.h>

typedef short bf16x8 __attribute__((ext_vector_type(8)));
typedef short s16x4 __attribute__((ext_vector_type(4)));
typedef float f32x16 __attribute__((ext_vector_type(16)));
typedef float f32x2 __attribute__((ext_vector_type(2)));
typedef __bf16 bf16x2_t __attribute__((ext_vector_type(2)));
typedef unsigned u32x4 __attribute__((ext_vector_type(4)));
typedef unsigned u32x2 __attribute__((ext_vector_type(2)));
#define AS3 __attribute__((address_space(3)))
#define DI __device__ __forceinline__

constexpr int HALF = 8192;
constexpr int RINGB = 9 * HALF;
constexpr float C2 = 0.088388347648318440f * 1.4426950408889634f;
#define PBOUND 1048576.0f

#define MFMA(a, b, c) __builtin_amdgcn_mfma_f32_32x32x16_bf16((a), (b), (c), 0, 0, 0)
#define LBAR() asm volatile("s_waitcnt lgkmcnt(0)\n\ts_barrier" ::: "memory")
#define SB() __builtin_amdgcn_sched_barrier(0)
#define PIN(x) asm volatile("" : "+v"(x))
#define GAPEND(acc) do { asm volatile("" : "+v"(acc) :: "memory"); SB(); } while (0)
#define MX3(a, b, c) __builtin_fmaxf(__builtin_fmaxf((a), (b)), (c))
#define EXP2(x) __builtin_amdgcn_exp2f(x)

DI unsigned cvtpk(float lo, float hi) {
    f32x2 v = {lo, hi};
    bf16x2_t b = __builtin_convertvector(v, bf16x2_t);
    return __builtin_bit_cast(unsigned, b);
}
template <int S> DI bf16x8 pack8(const f32x16& x) {
    u32x4 p = {cvtpk(x[8 * S + 0], x[8 * S + 1]), cvtpk(x[8 * S + 2], x[8 * S + 3]),
               cvtpk(x[8 * S + 4], x[8 * S + 5]), cvtpk(x[8 * S + 6], x[8 * S + 7])};
    return __builtin_bit_cast(bf16x8, p);
}
DI bf16x8 lds128(unsigned a) { return *(const AS3 bf16x8*)(size_t)(a); }
DI s16x4 ldstr(unsigned a) {
    return __builtin_bit_cast(s16x4, __builtin_amdgcn_ds_read_tr16_b64_v4i16((AS3 s16x4*)(size_t)(a)));
}
DI bf16x8 cat(s16x4 lo, s16x4 hi) { return __builtin_shufflevector(lo, hi, 0, 1, 2, 3, 4, 5, 6, 7); }
DI bf16x8 ldtr2(unsigned a0, unsigned a1) { return cat(ldstr(a0), ldstr(a1)); }
DI float swapmax(float x) {
    auto r = __builtin_amdgcn_permlane32_swap(__float_as_uint(x), __float_as_uint(x), false, false);
    return __builtin_fmaxf(__uint_as_float(r[0]), __uint_as_float(r[1]));
}
DI float swapsum(float x) {
    auto r = __builtin_amdgcn_permlane32_swap(__float_as_uint(x), __float_as_uint(x), false, false);
    return __uint_as_float(r[0]) + __uint_as_float(r[1]);
}
DI float max16(const f32x16& s) {
    float a = MX3(s[0], s[1], s[2]), b = MX3(s[3], s[4], s[5]);
    a = MX3(a, s[6], s[7]); b = MX3(b, s[8], s[9]);
    a = MX3(a, s[10], s[11]); b = MX3(b, s[12], s[13]);
    a = MX3(a, s[14], s[15]);
    return __builtin_fmaxf(a, b);
}
DI unsigned lane_id() { return __builtin_amdgcn_mbcnt_hi(~0u, __builtin_amdgcn_mbcnt_lo(~0u, 0u)); }

#define M0SET(base, off) asm volatile("s_add_u32 m0, %0, %1" :: "s"(base), "i"(off) : "scc")
#define GLD(voff, sb, imm) asm volatile("global_load_lds_dwordx4 %0, %1 offset:%2" :: "v"(voff), "s"(sb), "i"(imm) : "memory")
#define GLDNT(voff, sb, imm) asm volatile("global_load_lds_dwordx4 %0, %1 offset:%2 nt" :: "v"(voff), "s"(sb), "i"(imm) : "memory")
#define NOP0() asm volatile("s_nop 0")

#define ARRIVE(N) ARRIVE2(N, 0)
#define ARRIVE2(N, LG) do { unsigned long long sv_; asm volatile( \
        "s_waitcnt vmcnt(" #N ") lgkmcnt(" #LG ")\n\t" \
        "s_mov_b64 %0, exec\n\t" \
        "s_mov_b64 exec, 1\n\t" \
        "ds_add_u32 %1, %1\n\t" \
        "s_mov_b64 exec, %0" : "=&s"(sv_) : "v"(cnt) : "memory"); arr += arrinc; } while (0)
DI void wait_cnt(unsigned cnt, unsigned target) {
    unsigned v, s;
    asm volatile(
        "1:\n\t"
        "ds_read_b32 %0, %2\n\t"
        "s_waitcnt lgkmcnt(0)\n\t"
        "v_readfirstlane_b32 %1, %0\n\t"
        "s_nop 0\n\t"
        "s_cmp_ge_u32 %1, %3\n\t"
        "s_cbranch_scc0 1b"
        : "=&v"(v), "=&s"(s) : "v"(cnt), "s"(target) : "memory", "scc");
}
#define POLL() (*(volatile AS3 unsigned*)(size_t)cnt)
#define CHECK(pv) do { if (__builtin_expect((unsigned)__builtin_amdgcn_readfirstlane(pv) < arr, 0)) wait_cnt(cnt, arr); \
                       asm volatile("" ::: "memory"); SB(); } while (0)

__global__ __launch_bounds__(512, 2) void fused_attn(const char* __restrict__ xA, const char* __restrict__ kA,
                                                     const char* __restrict__ vA, const char* __restrict__ wqA,
                                                     const char* __restrict__ woA, const char* __restrict__ xB,
                                                     const char* __restrict__ kB, const char* __restrict__ vB,
                                                     const char* __restrict__ wqB, const char* __restrict__ woB,
                                                     char* __restrict__ part) {
    __shared__ __attribute__((aligned(1024))) char lds[2 * RINGB + 1024 + 2048];
    const int tid = threadIdx.x;
    const int wave = __builtin_amdgcn_readfirstlane(tid >> 6);
    const int g = wave >> 2, wr = wave & 3;
    const int bid = blockIdx.x;

    const unsigned lds0 = (unsigned)(size_t)(AS3 char*)lds;
    const unsigned ring = lds0 + g * RINGB;
    const unsigned mbase = ring + wr * 2048;
    unsigned cnt = lds0 + 2 * RINGB + g * 128;
    const unsigned arrinc = 4 * cnt;
    PIN(cnt);
    if (tid < 64) *(AS3 unsigned*)(size_t)(lds0 + 2 * RINGB + tid * 4) = 0;

    const unsigned flagw = lds0 + 2 * RINGB + 64;
#pragma unroll 1
    for (int pass = 0; pass < 2; ++pass) {
    unsigned arr = 0;
    int bid_ = bid; asm volatile("" : "+s"(bid_));
    const int hg = bid_ & 7, idx = bid_ >> 3, blk = idx >> 4, b = idx & 15;
    const char* X = (blk ? xB : xA) + (size_t)b * 131072 + wr * 32768;
    const char* WQ = (blk ? wqB : wqA) + (size_t)wr * 65536 + (4 * hg + g) * 256;
    const char* KK = (blk ? kB : kA) + ((size_t)b * 32 + 4 * hg + g) * 131072 + wr * 2048;
    const char* VV = (blk ? vB : vA) + ((size_t)b * 32 + 4 * hg + g) * 131072 + wr * 2048;
    const char* WO = (blk ? woB : woA) + (size_t)(4 * hg) * 131072 + wr * 8192 + g * 512;

    unsigned lane = lane_id(); PIN(lane);
    const unsigned r32 = lane & 31, h = lane >> 5;
    const unsigned colb = (unsigned)(h * 64 + 16 * ((lane & 3) ^ ((2 * wr + ((lane >> 4) & 1)) & 3)));
    const unsigned rowl = (lane >> 2) & 7;
    const unsigned vWQ = rowl * 8192 + colb;
    const unsigned vX = (lane >> 2) * 1024 + 16 * ((lane & 3) ^ ((lane >> 4) & 3)), vX2 = vX + 16384;
    const unsigned blkb = (lane >> 4) & 1, q4 = (lane & 15) >> 2, p4 = lane & 3;
    const unsigned xr = (r32 >> 2) & 3;
    unsigned rdK0 = ring + 2048 * (r32 >> 3) + 64 * (r32 & 7) + 16 * (h ^ xr), rdK1 = rdK0 ^ 32;
    unsigned rdV0 = ring + 64 * (4 * h + q4) + 16 * ((2 * blkb + (p4 >> 1)) ^ h) + 8 * (p4 & 1), rdV1 = rdV0 ^ 32;
    const unsigned rdW0 = ring + 2048 * h + 64 * q4 + 16 * ((2 * blkb + (p4 & 1)) ^ (2 * h)) + 8 * (p4 >> 1), rdW1 = rdW0 ^ 16;
    const unsigned rdW0x = rdW0 + 8 * HALF, rdW1x = rdW1 + 8 * HALF;
    const unsigned rdX0 = ring + wr * 2048 + 64 * r32 + 16 * (h ^ xr), rdX1 = rdX0 ^ 32;

    unsigned vT = rowl * 256 + colb, vT2 = vT + 8192;

    bf16x8 R[4], xf0, xf1;
#define WRD(s, ks, j) ((s) == 8 ? ldtr2(rdW0x + 4096 * (ks) + 512 * (j), rdW1x + 4096 * (ks) + 512 * (j) + 256) \
                                : ldtr2(rdW0 + (s) * HALF + 4096 * (ks) + 512 * (j), rdW1 + (s) * HALF + 4096 * (ks) + 512 * (j) + 256))
#define KRD(s, d) (((d) & 1) ? lds128(rdK1 + (s) * HALF + 512 * ((d) >> 1)) : lds128(rdK0 + (s) * HALF + 512 * ((d) >> 1)))
#define VRD(s, k1, j) ldtr2(rdV0 + (s) * HALF + 4096 * (k1) + 512 * (j), rdV1 + (s) * HALF + 4096 * (k1) + 2048 + 512 * (j))

#define QFR(s, i) WRD((s) + 1 + ((i) >> 3), ((i) >> 2) & 1, (i) & 3)
#define KFR(s, i) KRD((s) + ((i) >> 3), (i) & 7)
#define VFR(s, i) VRD((s) + ((i) >> 3), ((i) >> 2) & 1, (i) & 3)

#pragma unroll
    for (int u = 0; u < 3; ++u) {
        const int s = 3 * ((u + 2) % 3);
        const char* px = X + u * 64; const char* pw = WQ + (size_t)u * 262144;
        M0SET(mbase, s * HALF); NOP0(); GLD(vX, px, 0); M0SET(mbase, s * HALF + 1024); NOP0(); GLD(vX2, px, 0);
        M0SET(mbase, (s + 1) * HALF); NOP0(); GLD(vWQ, pw, 0); M0SET(mbase, (s + 1) * HALF + 896); NOP0(); GLD(vWQ, pw, 128);
        M0SET(mbase, (s + 2) * HALF - 512); NOP0(); GLD(vWQ, pw, 512); M0SET(mbase, (s + 2) * HALF + 896 - 512); NOP0(); GLD(vWQ, pw, 640);
    }
    LBAR();
    ARRIVE(12);
    wait_cnt(cnt, arr);
    {
        const unsigned mw = lds0 + 2 * RINGB + 1024 + wave * 256, vl = lane * 64;
        const char* w2 = (blk ? woB : woA) + (size_t)(4 * hg) * 131072 + b * 32768 + wave * 4096;
        asm volatile("s_mov_b32 m0, %0" :: "s"(mw)); NOP0();
        asm volatile("global_load_lds_dword %0, %1" :: "v"(vl), "s"(w2) : "memory");
    }
    xf0 = lds128(rdX0 + 6 * HALF);
#pragma unroll
    for (int j = 0; j < 3; ++j) R[j] = QFR(6, j);

    bf16x8 qf[8], sh[8];
    {
        f32x16 qa[4] = {}, qb[4] = {};
        const char* px = X + 3 * 64; const char* pw = WQ + (size_t)3 * 262144;
#define QM0(MODE, sp, p) do { if (MODE == 1) { \
                    M0SET(mbase, (p) == 0 ? (sp) * HALF : (p) == 1 ? (sp) * HALF + 1024 : (p) == 2 ? ((sp) + 1) * HALF : (p) == 3 ? ((sp) + 1) * HALF + 896 \
                          : (p) == 4 ? ((sp) + 2) * HALF - 512 : ((sp) + 2) * HALF + 896 - 512); \
                } else if (MODE >= 2) { M0SET(mbase, ((sp) + ((p) >> 1)) * HALF + ((p) & 1) * 896); } } while (0)
#define QLD(MODE, p) do { if (MODE == 1) { \
                    if ((p) == 0) GLD(vX, px, 0); else if ((p) == 1) GLD(vX2, px, 0); else if ((p) == 2) GLD(vWQ, pw, 0); \
                    else if ((p) == 3) GLD(vWQ, pw, 128); else if ((p) == 4) GLD(vWQ, pw, 512); else GLD(vWQ, pw, 640); \
                } else if (MODE == 2) { \
                    if ((p) < 2) GLDNT(vT, KK, ((p) & 1) * 128); else if ((p) < 4) GLDNT(vT2, KK, ((p) & 1) * 128); else GLDNT(vT, VV, ((p) & 1) * 128); \
                } else if (MODE == 3) { \
                    const char* k1_ = KK + 16384; \
                    if ((p) < 2) GLDNT(vT2, VV, ((p) & 1) * 128); else if ((p) < 4) GLDNT(vT, k1_, ((p) & 1) * 128); else GLDNT(vT2, k1_, ((p) & 1) * 128); \
                } } while (0)
#define QP(acc, MODE, p) do { if (MODE) { PIN(acc); QLD(MODE, p); } } while (0)
#define QSTEP(U3, MODE) do { \
            constexpr int s = 3 * (((U3) + 2) % 3), sp = 3 * (((U3) + 1) % 3), sn = 3 * ((U3) % 3); \
            unsigned pv; \
            QM0(MODE, sp, 0); SB(); \
            xf1 = lds128(rdX1 + s * HALF); \
            _Pragma("unroll") for (int i = 0; i < 16; ++i) { \
                const bf16x8 cur = R[i & 3]; \
                if (i == 13) { ARRIVE(6); pv = POLL(); } \
                if (i == 15) { CHECK(pv); \
                    if (MODE != 3) { xf0 = lds128(rdX0 + sn * HALF); _Pragma("unroll") for (int j = 0; j < 3; ++j) R[j] = QFR(sn, j); } \
                    else { _Pragma("unroll") for (int j = 0; j < 3; ++j) R[j] = KFR(0, j); } } \
                if (i < 13) R[(i + 3) & 3] = QFR(s, i + 3); \
                if (i < 8) { qa[i & 3] = MFMA(cur, (i & 4) ? xf1 : xf0, qa[i & 3]); \
                    if (!(i & 1)) QP(qa[i & 3], MODE, i >> 1); else QM0(MODE, sp, (i >> 1) + 1); \
                    GAPEND(qa[i & 3]); } \
                else { qb[i & 3] = MFMA(cur, (i & 4) ? xf1 : xf0, qb[i & 3]); \
                    if (i < 12) { if (!(i & 1)) QP(qb[i & 3], MODE, i >> 1); else if (i == 9) QM0(MODE, sp, 5); } \
                    GAPEND(qb[i & 3]); } \
            } \
            if (MODE == 1) { px += 64; pw += 262144; } \
        } while (0)
        QSTEP(0, 0);
#pragma unroll 1
        for (int i = 0; i < 4; ++i) { QSTEP(1, 1); QSTEP(2, 1); QSTEP(0, 1); }
        QSTEP(1, 1); QSTEP(2, 2); QSTEP(0, 3);
#pragma unroll
        for (int dt = 0; dt < 4; ++dt) {
#pragma unroll
            for (int i = 0; i < 16; ++i) { qa[dt][i] *= C2; qb[dt][i] *= C2; }
            qf[2 * dt] = pack8<0>(qa[dt]); qf[2 * dt + 1] = pack8<1>(qa[dt]);
            sh[2 * dt] = pack8<0>(qb[dt]); sh[2 * dt + 1] = pack8<1>(qb[dt]);
        }
    }

    f32x16 o[4];
    f32x16 ng;
    float l = 0.f;
    const char* pV = VV + 16384;
    const char* pK = KK + 32768;
    unsigned mcur = mbase, moth = mbase + 4 * HALF;
    int dlt = 4 * HALF;
#define PF(w) __builtin_bit_cast(bf16x8, w)
#define TM0(MB, SL, j) M0SET(MB, ((SL) + ((j) >> 1)) * HALF + ((j) & 1) * 896)
#define TLD(P, j) do { if ((j) < 2) GLDNT(vT, P, ((j) & 1) * 128); else GLDNT(vT2, P, ((j) & 1) * 128); } while (0)
#define TLD_WO(P, j) do { if ((j) < 2) GLD(vT, P, ((j) & 1) * 128); else GLD(vT2, P, ((j) & 1) * 128); } while (0)
#define EXPK(S, B, PW, j) do { S[(B) + 2 * (j)] = EXP2(S[(B) + 2 * (j)]); S[(B) + 2 * (j) + 1] = EXP2(S[(B) + 2 * (j) + 1]); \
                               PW[j] = cvtpk(S[(B) + 2 * (j)], S[(B) + 2 * (j) + 1]); } while (0)
#define ADD2(S, B, j) do { ls += S[(B) + 2 * (j)]; ls += S[(B) + 2 * (j) + 1]; } while (0)
#pragma unroll 1
    for (int hd = 0; hd < 2; ++hd) {
        {
            f32x16 s0 = {};
#pragma unroll
            for (int j = 0; j < 8; ++j) { const bf16x8 kf = j < 3 ? R[j] : KFR(0, j); s0 = MFMA(kf, qf[j], s0); }
            float mx = max16(s0);
            if (pass) {
                const unsigned ln = lane_id();
                const char* kp = KK - wr * 2048 + hd * 262144 + (ln & 31) * 256 + (ln >> 5) * 16;
#pragma unroll 1
                for (int kb = 1; kb < 16; ++kb) {
                    s0 = f32x16{};
#pragma unroll
                    for (int d = 0; d < 8; ++d) s0 = MFMA(*(const bf16x8*)(kp + kb * 8192 + d * 32), qf[d], s0);
                    mx = __builtin_fmaxf(mx, max16(s0));
                }
            }
            const float m0_ = swapmax(mx);
#pragma unroll
            for (int i = 0; i < 16; ++i) ng[i] = -m0_;
            asm volatile("" : "+v"(ng) :: "memory"); SB();
        }
#pragma unroll
        for (int dt = 0; dt < 4; ++dt) o[dt] = f32x16{};
        l = 0.f;
#pragma unroll 1
    for (int tt = 8 * hd; tt < 8 * hd + 8; ++tt) {
        f32x16 s0, s1;
        u32x4 pw0, pw1, pw2, pw3;
        float ls = 0.f;
        {
            unsigned pv;
#pragma unroll
            for (int i = 0; i < 16; ++i) {
                const bf16x8 cur = R[i & 3];
                if (i == 3) pv = POLL();
                if (i == 5) { CHECK(pv); TM0(moth, 2, 0); SB(); }
                if (i == 14) ARRIVE2(5, 2);
                R[(i + 3) & 3] = i < 13 ? KFR(0, i + 3) : VFR(2, i - 13);
                if (i < 8) {
                    s0 = MFMA(cur, qf[i], i == 0 ? ng : s0);
                    if (i == 5) { PIN(s0); TLD(pV, 0); } if (i == 6) TM0(moth, 2, 1); if (i == 7) { PIN(s0); TLD(pV, 1); }
                    GAPEND(s0);
                } else {
                    s1 = MFMA(cur, qf[i - 8], i == 8 ? ng : s1);
                    if (i == 8) TM0(moth, 2, 2);
                    if (i == 9) { PIN(s0); EXPK(s0, 0, pw0, 0); EXPK(s0, 0, pw0, 1); asm volatile("" : "+v"(s0), "+v"(pw0)); TLD(pV, 2); }
                    if ((i == 10 || i == 11)) { EXPK(s0, 0, pw0, i - 8); asm volatile("" : "+v"(s0), "+v"(pw0)); }
                    if (i >= 12) { EXPK(s0, 8, pw1, i - 12); ADD2(s0, 0, i - 12); if (i > 12) ADD2(s0, 8, i - 13);
                                   asm volatile("" : "+v"(s0), "+v"(pw1), "+v"(ls)); }
                    if (i == 13) { rdK0 += dlt; rdK1 += dlt; }
                    if (i == 14) TM0(moth, 2, 3); if (i == 15) TLD(pV, 3);
                    GAPEND(s1);
                }
            }
            ADD2(s0, 8, 3);
        }
        if (tt == 14) { vT = (vT & 0xff) | ((vT & 0x700) << 2); vT2 = vT + 32768; pK = WO + g * 131072; }
        {
            unsigned pv;
#pragma unroll
            for (int i = 0; i < 16; ++i) {
                const bf16x8 cur = R[i & 3];
                if (i == 3) pv = POLL();
                if (i == 5) { CHECK(pv); TM0(mcur, 0, 0); SB(); }
                if (i == 8) { ADD2(s1, 8, 3); l += ls; }
                if (i == 14) ARRIVE2(5, 1);
                R[(i + 3) & 3] = i < 13 ? VFR(2, i + 3) : KFR(0, i - 13);
                o[i & 3] = MFMA(cur, i < 4 ? PF(pw0) : i < 8 ? PF(pw1) : i < 12 ? PF(pw2) : PF(pw3), o[i & 3]);
                if (i == 1) { PIN(s1); EXPK(s1, 0, pw2, 0); EXPK(s1, 0, pw2, 1); asm volatile("" : "+v"(s1), "+v"(pw2)); }
                if ((i == 2 || i == 3)) { EXPK(s1, 0, pw2, i); asm volatile("" : "+v"(s1), "+v"(pw2)); }
                if (i >= 4 && i < 8) { EXPK(s1, 8, pw3, i - 4); ADD2(s1, 0, i - 4); if (i > 4) ADD2(s1, 8, i - 5);
                                       asm volatile("" : "+v"(s1), "+v"(pw3), "+v"(ls)); }
                if (i == 5) TLD(pK, 0); if (i == 7) TM0(mcur, 0, 1);
                if (i == 8) { PIN(o[i & 3]); TLD(pK, 1); } if (i == 10) TM0(mcur, 0, 2); if (i == 11) { PIN(o[i & 3]); TLD(pK, 2); }
                if (i == 13) { rdV0 += dlt; rdV1 += dlt; }
                if (i == 14) TM0(mcur, 0, 3); if (i == 15) { PIN(o[i & 3]); TLD(pK, 3); }
                GAPEND(o[i & 3]);
            }
        }
        pV = (tt == 14) ? pK + 65536 : pV + ((tt == 6) ? 16384 + 131072 : 16384);
        pK += (tt == 14) ? 262144 : (tt == 5) ? 16384 + 131072 : 16384;
        { const unsigned t_ = mcur; mcur = moth; moth = t_; dlt = -dlt; }
    }
        {
            const float lt = swapsum(l);
            if (!(lt <= PBOUND)) *(volatile AS3 unsigned*)(size_t)flagw = 1u;
            const float inv = __builtin_amdgcn_rcpf(lt);
#pragma unroll
            for (int dt = 0; dt < 4; ++dt) {
#pragma unroll
                for (int i = 0; i < 16; ++i) o[dt][i] *= inv;
                qf[2 * dt] = sh[2 * dt]; qf[2 * dt + 1] = sh[2 * dt + 1];
                sh[2 * dt] = pack8<0>(o[dt]); sh[2 * dt + 1] = pack8<1>(o[dt]);
            }
        }
    }
    bf16x8 pt0[8], pt1[8];
    unsigned vP = lane_id() * 16; PIN(vP);
    const char* wown = WO + g * 131072; const char* wpar = WO + (1 - g) * 131072;
#define WOT(c_, kt_) ((((kt_) >> 2) ? wpar : wown) + (((kt_) >> 1) & 1) * 262144 + ((kt_) & 1) * 65536 + (c_) * 256)
    {
        ARRIVE(0);
        wait_cnt(cnt, arr);
        const unsigned e = wr * 2 * HALF + vP;
#pragma unroll
        for (int f = 0; f < 8; ++f) {
            *(AS3 bf16x8*)(size_t)(ring + e + f * 1024) = qf[f];
            *(AS3 bf16x8*)(size_t)(ring + e + HALF + f * 1024) = sh[f];
        }
        LBAR();
        const unsigned oring = lds0 + (1 - g) * RINGB;
#pragma unroll
        for (int f = 0; f < 8; ++f) { pt0[f] = lds128(oring + e + f * 1024); pt1[f] = lds128(oring + e + HALF + f * 1024); }
        LBAR();
#pragma unroll
        for (int t = 0; t < 3; ++t) {
            const char* w = WOT(0, t);
#pragma unroll
            for (int j = 0; j < 4; ++j) { TM0(mbase, 2 * t, j); NOP0(); TLD_WO(w, j); }
        }
        ARRIVE(6);
        wait_cnt(cnt, arr);
#pragma unroll
        for (int j = 0; j < 3; ++j) R[j] = VFR(0, j);
    }

    char* pw = part + ((size_t)(idx * 8 + hg) * 4 + wr) * 32768 + g * 16384;
#pragma unroll 1
    for (int c = 0; c < 2; ++c) {
        f32x16 oa[4] = {};
#define OSTEP(kt, OF) do { \
            constexpr int B = (2 * (kt)) & 7; \
            asm volatile("" : "+s"(wown), "+s"(wpar));                     \
            const char* wn = (kt) < 5 ? WOT(c, (kt) + 3) : WOT(1, (kt) - 5);     \
            unsigned pv; \
            _Pragma("unroll") for (int i = 0; i < 16; ++i) { \
                const bf16x8 cur = R[i & 3]; \
                if (i == 3) pv = POLL(); \
                if (i == 5) { CHECK(pv); TM0(mbase, (B + 6) & 7, 0); SB(); } \
                if (i == 14) { if ((kt) == 0) { if (c == 0) ARRIVE2(5, 2); else ARRIVE2(13, 2); } else ARRIVE2(5, 2); } \
                R[(i + 3) & 3] = i < 13 ? VFR(B, i + 3) : VFR((B + 2) & 7, i - 13); \
                oa[i & 3] = MFMA(cur, OF[((kt) & 1) * 4 + (i >> 2)], oa[i & 3]); \
                if (i == 5 || i == 8 || i == 11 || i == 15) { PIN(oa[i & 3]); TLD_WO(wn, i == 5 ? 0 : i == 8 ? 1 : i == 11 ? 2 : 3); } \
                if (i == 7) TM0(mbase, (B + 6) & 7, 1); if (i == 10) TM0(mbase, (B + 6) & 7, 2); if (i == 14) TM0(mbase, (B + 6) & 7, 3); \
                GAPEND(oa[i & 3]); \
            } } while (0)
        OSTEP(0, qf); OSTEP(1, qf); OSTEP(2, sh); OSTEP(3, sh); OSTEP(4, pt0); OSTEP(5, pt0); OSTEP(6, pt1); OSTEP(7, pt1);
#pragma unroll
        for (int nt = 0; nt < 4; ++nt) {
            *(bf16x8*)(pw + (nt * 2 + 0) * 1024 + vP) = pack8<0>(oa[nt]);
            *(bf16x8*)(pw + (nt * 2 + 1) * 1024 + vP) = pack8<1>(oa[nt]);
        }
        asm volatile("" ::: "memory"); SB();
        pw += 8192;
    }
    asm volatile("s_waitcnt vmcnt(0)" ::: "memory");
    LBAR();
    if (pass == 1 || __builtin_amdgcn_readfirstlane(*(volatile AS3 unsigned*)(size_t)flagw) == 0) break;
    LBAR();
    if (wave == 0) *(AS3 unsigned*)(size_t)(lds0 + 2 * RINGB + lane_id() * 4) = 0;
    }
}

__global__ __launch_bounds__(256) void reduce_out(const char* __restrict__ part, unsigned short* __restrict__ out) {
    __shared__ __attribute__((aligned(16))) char tile[32 * 272];
    const int tid = threadIdx.x, lane = tid & 63, w = tid >> 6;
    const int c = blockIdx.x & 3, wr = (blockIdx.x >> 2) & 3, idx = blockIdx.x >> 4;
    const char* src = part + ((size_t)(idx * 8) * 4 + wr) * 32768 + (c * 8 + w * 2) * 1024 + lane * 16;
    u32x4 v[2][8];
#pragma unroll
    for (int hg = 0; hg < 8; ++hg) {
        v[0][hg] = *(const u32x4*)(src + (size_t)hg * 131072);
        v[1][hg] = *(const u32x4*)(src + (size_t)hg * 131072 + 1024);
    }
#pragma unroll
    for (int s = 0; s < 2; ++s) {
        float acc[8] = {};
#pragma unroll
        for (int hg = 0; hg < 8; ++hg)
#pragma unroll
            for (int j = 0; j < 4; ++j) {
                acc[2 * j] += __uint_as_float(v[s][hg][j] << 16);
                acc[2 * j + 1] += __uint_as_float(v[s][hg][j] & 0xffff0000u);
            }
        char* t = tile + (lane & 31) * 272 + (32 * w + 16 * s + 4 * (lane >> 5)) * 2;
        *(u32x2*)(t) = u32x2{cvtpk(acc[0], acc[1]), cvtpk(acc[2], acc[3])};
        *(u32x2*)(t + 16) = u32x2{cvtpk(acc[4], acc[5]), cvtpk(acc[6], acc[7])};
    }
    __syncthreads();
    const int blk = idx >> 4, b = idx & 15;
#pragma unroll
    for (int p = 0; p < 2; ++p) {
        const int row = p * 16 + (tid >> 4), ch = tid & 15;
        const u32x4 o = *(const u32x4*)(tile + row * 272 + ch * 16);
        *(u32x4*)((char*)out + ((size_t)(b * 128 + wr * 32 + row) * 1024 + blk * 512 + c * 128) * 2 + ch * 16) = o;
    }
}

extern "C" void kernel_launch(void* const* d_in, const int* in_sizes, int n_in, void* d_out, int out_size, void* d_ws,
                              size_t ws_size, hipStream_t stream) {
    char* part = (char*)d_ws;
    fused_attn<<<256, 512, 0, stream>>>((const char*)d_in[0], (const char*)d_in[1], (const char*)d_in[2],
                                        (const char*)d_in[3], (const char*)d_in[4], (const char*)d_in[5],
                                        (const char*)d_in[6], (const char*)d_in[7], (const char*)d_in[8],
                                        (const char*)d_in[9], part);
    reduce_out<<<512, 256, 0, stream>>>(part, (unsigned short*)d_out);
}
```
